```python
import torch

def _softmax(x, axis=-1):
    return torch.softmax(x, dim=axis)

def _gelu(x):
    return torch.nn.functional.gelu(x, approximate='tanh')

def _silu(x):
    return torch.nn.functional.silu(x)

def _sigmoid(x):
    return torch.sigmoid(x)

def _layernorm(x, axis=-1, eps=1e-05):
    m = x.mean(dim=axis, keepdim=True)
    v = x.var(dim=axis, keepdim=True, unbiased=False)
    return (x - m) / torch.sqrt(v + eps)

def _scatter_add(tgt, idx, src, axis=0):
    return tgt.clone().index_add_(axis, idx, src)

def _segment_sum(src, seg, n):
    out = torch.zeros((n,) + src.shape[1:], dtype=src.dtype)
    return out.index_add_(0, seg, src)

def _pad(x, pw):
    flat = tuple((v for pair in reversed(pw) for v in pair))
    return torch.nn.functional.pad(x, flat)
kernel_name = 'standalone__varlen_attn_packed__ragged'

def setup_inputs():

    _w = lambda a: (lambda t: t.to(torch.bfloat16) if t.is_floating_point() else t)(torch.as_tensor(a))
    cu = torch.tensor([0, 238, 376, 669, 835, 1320, 1545, 1865, 2058], dtype=torch.int64)
    T, H, D = (int(cu[-1]), 16, 192)
    return {'q': _w((lambda *s: torch.randn(*s, dtype=torch.bfloat16) * 0.8)(T, H, D)), 'k': _w((lambda *s: torch.randn(*s, dtype=torch.bfloat16) * 0.8)(T, H, D)), 'v': _w((lambda *s: torch.randn(*s, dtype=torch.bfloat16) * 0.05)(T, H, D)), 'cu': _w(cu)}

def reference(q, k, v, cu):
    T, H, D = q.shape
    out = torch.zeros_like(q)
    scale = D ** (-0.5)
    outs = []
    for i in range(len(cu) - 1):
        s, e = (int(cu[i]), int(cu[i + 1]))
        qi, ki, vi = (q[s:e].clone(), k[s:e].clone(), v[s:e].clone())
        p = _softmax(torch.einsum('qhd,khd->hqk', qi, ki) * scale, axis=-1)
        outs.append(torch.einsum('hqk,khd->qhd', p, vi))
    return torch.concatenate(outs, axis=0)
```

```cpp
#include <hip/hip_runtime.h>
#include <hip/hip_bf16.h>
#include <stdint.h>

typedef short    bf16x8 __attribute__((ext_vector_type(8)));
typedef short    s16x4  __attribute__((ext_vector_type(4)));
typedef float    f32x16 __attribute__((ext_vector_type(16)));
typedef float    f32x4  __attribute__((ext_vector_type(4)));
typedef float    f32x2  __attribute__((ext_vector_type(2)));
typedef __bf16   bf16x2 __attribute__((ext_vector_type(2)));
typedef unsigned u32x4  __attribute__((ext_vector_type(4)));
typedef __attribute__((address_space(3))) char* lds_ptr;

#define DI __device__ __forceinline__
constexpr int H = 16, D = 192, ROWB = H * D * 2, HEADB = D * 2;
constexpr int NWG = 256, GPH = NWG / H;
constexpr int BLKB = 12288;
constexpr int SLOTB = 2 * BLKB, NSLOT = 6;
constexpr int LDSB = 163840;
constexpr float CEXP = 0.07216878364870323f * 1.4426950408889634f;
constexpr float THR = 8.0f;

#define MFMA(a, b, c) __builtin_amdgcn_mfma_f32_32x32x16_bf16(a, b, c, 0, 0, 0)

DI unsigned cvtpk(float lo, float hi) { f32x2 v = {lo, hi}; bf16x2 b = __builtin_convertvector(v, bf16x2); return __builtin_bit_cast(unsigned, b); }
DI int rfl(int x) { return __builtin_amdgcn_readfirstlane(x); }

DI void dma3(const void* base, unsigned voff, unsigned ldsb) {
  unsigned keep;
  asm volatile(
      "s_mov_b32 %0, m0\n\t"
      "s_mov_b32 m0, %3\n\t"
      "s_nop 0\n\t"
      "global_load_lds_dwordx4 %1, %2\n\t"
      "s_add_u32 m0, m0, 896\n\t"
      "s_nop 0\n\t"
      "global_load_lds_dwordx4 %1, %2 offset:128\n\t"
      "s_add_u32 m0, m0, 896\n\t"
      "s_nop 0\n\t"
      "global_load_lds_dwordx4 %1, %2 offset:256\n\t"
      "s_mov_b32 m0, %0"
      : "=&s"(keep)
      : "v"(voff), "s"(base), "s"(ldsb)
      : "memory", "scc");
}

DI void wait_vm(int n3) {
  if (n3 <= 0)      asm volatile("s_waitcnt vmcnt(0)" ::: "memory");
  else if (n3 == 1) asm volatile("s_waitcnt vmcnt(3)" ::: "memory");
  else if (n3 == 2) asm volatile("s_waitcnt vmcnt(6)" ::: "memory");
  else if (n3 == 3) asm volatile("s_waitcnt vmcnt(9)" ::: "memory");
  else if (n3 == 4) asm volatile("s_waitcnt vmcnt(12)" ::: "memory");
  else              asm volatile("s_waitcnt vmcnt(15)" ::: "memory");
}
#define BARRIER() asm volatile("s_waitcnt lgkmcnt(0)\n\ts_barrier" ::: "memory")

DI bf16x8 lds_rd128(unsigned a) { return *(const __attribute__((address_space(3))) bf16x8*)(uintptr_t)a; }
DI s16x4 lds_tr(unsigned a) { return __builtin_amdgcn_ds_read_tr16_b64_v4i16((__attribute__((address_space(3))) s16x4*)(uintptr_t)a); }

DI void store3(const f32x16& o0, const f32x16& o1, const f32x16& o2, char* rowp, bool ok) {
#pragma unroll
  for (int b = 0; b < 3; ++b) {
    const f32x16& o = b == 0 ? o0 : (b == 1 ? o1 : o2);
#pragma unroll
    for (int g = 0; g < 4; g += 2) {
      unsigned ax = cvtpk(o[4 * g + 0], o[4 * g + 1]), ay = cvtpk(o[4 * g + 2], o[4 * g + 3]);
      unsigned bx = cvtpk(o[4 * g + 4], o[4 * g + 5]), by = cvtpk(o[4 * g + 6], o[4 * g + 7]);
      auto rx = __builtin_amdgcn_permlane32_swap(ax, bx, false, false);
      auto ry = __builtin_amdgcn_permlane32_swap(ay, by, false, false);
      u32x4 w = {rx[0], ry[0], rx[1], ry[1]};
      if (ok) *(u32x4*)(rowp + b * 64 + g * 16) = w;
    }
  }
}

__global__ __launch_bounds__(512, 2) void varlen_attn_kernel(const char* __restrict__ Q, const char* __restrict__ K, const char* __restrict__ V,
                                                             const int* __restrict__ cu, char* __restrict__ O, int nseq) {
  __shared__ __attribute__((aligned(1024))) char smem[LDSB];
  const unsigned lds0 = (unsigned)(uintptr_t)(lds_ptr)smem;
  const int tid = threadIdx.x, lane = tid & 63, w = rfl(tid >> 6);
  const int r32 = lane & 31, hi = lane >> 5;
  const int bid = blockIdx.x, head = (bid & 7) * 2 + ((bid >> 3) & 1), g0 = bid >> 4;
  const int cu0 = cu[0];

  int thrC;
  {
    int cnt = 0, nmax = 0;
    for (int i0 = 0; i0 < nseq; i0 += 64) { int i = i0 + lane; int n = i < nseq ? (cu[i + 1] - cu[i] + 31) >> 5 : 0; nmax = max(nmax, n); }
#pragma unroll
    for (int off = 32; off >= 1; off >>= 1) nmax = max(nmax, __shfl_xor(nmax, off));
    const int cand = nmax * ((lane >> 3) + 1);
    for (int i0 = 0; i0 < nseq; i0 += 8) {
      int i = i0 + (lane & 7); int n = i < nseq ? (cu[i + 1] - cu[i] + 31) >> 5 : 0;
      int sh = (8 * n <= cand) ? 3 : ((4 * n <= cand) ? 2 : 1);
      cnt += (n + (1 << sh) - 1) >> sh;
    }
    cnt += __shfl_xor(cnt, 1); cnt += __shfl_xor(cnt, 2); cnt += __shfl_xor(cnt, 4);
    unsigned long long okm = __ballot(cnt <= GPH);
    int c = okm ? (__builtin_ctzll(okm) >> 3) : 7;
    thrC = rfl(nmax) * (c + 1);
  }

  const int prow = lane >> 3;
  const int rg = w & 3;
  const unsigned swzK = (unsigned)(((lane & 7) ^ ((4 * rg + (prow >> 1)) & 7)) * 16);
  const unsigned swzV = (unsigned)(((lane & 7) ^ (((prow >> 1) & 1) << 2)) * 16);
  const unsigned swzKV = (w >= 4) ? swzV : swzK;
  unsigned kaddr[4];
#pragma unroll
  for (int s = 0; s < 4; ++s) kaddr[s] = lds0 + (r32 >> 3) * 3072 + (r32 & 7) * 128 + (((2 * s + hi) ^ ((r32 >> 1) & 7)) * 16);
  unsigned vaddr[2];
  { const int q4 = (lane & 15) >> 2, p4 = lane & 3, ch = (lane >> 4) & 1;
    vaddr[0] = lds0 + BLKB + (4 * hi + q4) * 128 + ((q4 >> 1) & 1) * 64 + ch * 32 + p4 * 8; vaddr[1] = vaddr[0] ^ 64; }

  int item = g0, seq = 0, base = 0;
  for (;;) {
    int s0 = 0, L = 0, nkb = 0, sh = 0, cnt = 0;
    for (; seq < nseq; ++seq) {
      s0 = cu[seq]; L = cu[seq + 1] - s0; nkb = (L + 31) >> 5;
      sh = (8 * nkb <= thrC) ? 3 : ((4 * nkb <= thrC) ? 2 : 1);
      cnt = (nkb + (1 << sh) - 1) >> sh;
      if (item < base + cnt) break;
      base += cnt;
    }
    if (seq >= nseq) break;
    const int it = item - base;
    const int qb0 = it << sh, nq = min(1 << sh, nkb - qb0);
    const int ks = nq <= 4 ? 2 : 1;
    int qi, kh, partner; bool act;
    if (ks == 1)      { qi = w; kh = 0; partner = w; act = w < nq; }
    else if (nq <= 2) { qi = w & 1; kh = (w >> 1) & 1; partner = w ^ 2; act = (w < 4) && (qi < nq); }
    else              { qi = w & 3; kh = w >> 2; partner = w ^ 4; act = qi < nq; }
    const int steps = (nkb + ks - 1) / ks, R = NSLOT / ks;
    const int rot = (it * steps) / cnt;
    const int tailn = L - (nkb - 1) * 32;
    const size_t hb = (size_t)s0 * ROWB + (size_t)head * HEADB;
    const char* kvbase = ((w >= 4) ? V : K) + hb;
    const unsigned voffN = (unsigned)(8 * rg + prow) * ROWB + swzKV;
    const unsigned voffT = (unsigned)min(8 * rg + prow, tailn - 1) * ROWB + swzKV;
    const unsigned kvdst = lds0 + ((w >= 4) ? BLKB : 0) + rg * 3072;

    auto issue = [&](int t) {
      int tt = t + rot; if (tt >= steps) tt -= steps;
      for (int j = 0; j < ks; ++j) {
        const int kb = min(tt * ks + j, nkb - 1);
        const int slot = (t * ks + j) % NSLOT;
        dma3(kvbase + (size_t)kb * (32 * ROWB), kb == nkb - 1 ? voffT : voffN, kvdst + slot * SLOTB);
      }
    };

    const unsigned qofs = (ks == 1) ? 2 * SLOTB : 4 * SLOTB;
    for (int g = w; g < nq * 4; g += 8) {
      const int row = min(qb0 * 32 + g * 8 + prow, L - 1);
      dma3(Q + hb, (unsigned)row * ROWB + swzK, lds0 + qofs + g * 3072);
    }
    int issued = min(2, steps);
    for (int t = 0; t < issued; ++t) issue(t);
    wait_vm((issued - 1) * ks);
    BARRIER();
    bf16x8 qf[12];
#pragma unroll
    for (int s = 0; s < 12; ++s) qf[s] = lds_rd128(kaddr[s & 3] + qofs + (act ? qi : 0) * BLKB + (s >> 2) * 1024);
    BARRIER();
    { const int e = min(R, steps); for (; issued < e; ++issued) issue(issued); }

    f32x16 o[6];
#pragma unroll
    for (int b = 0; b < 6; ++b) o[b] = f32x16{};
    float m = -INFINITY, l = 0.f;

    for (int t = 0; t < steps; ++t) {
      if (t > 0) {
        wait_vm((issued - 1 - t) * ks);
        BARRIER();
        if (issued < steps) { issue(issued); ++issued; }
      }
      int tt = t + rot; if (tt >= steps) tt -= steps;
      const int kb = tt * ks + kh;
      if (act && kb < nkb) {
        const unsigned so = (unsigned)((t * ks + kh) % NSLOT) * SLOTB;
        f32x16 s = f32x16{};
#pragma unroll
        for (int i = 0; i < 12; ++i) s = MFMA(lds_rd128(kaddr[i & 3] + so + (i >> 2) * 1024), qf[i], s);
        if (kb == nkb - 1 && tailn < 32) {
#pragma unroll
          for (int i = 0; i < 16; ++i) if ((i & 3) + 8 * (i >> 2) + 4 * hi >= tailn) s[i] = -INFINITY;
        }
        float mx = fmaxf(fmaxf(s[0], s[1]), s[2]);
#pragma unroll
        for (int i = 3; i < 15; i += 2) mx = fmaxf(fmaxf(mx, s[i]), s[i + 1]);
        mx = fmaxf(mx, s[15]);
        { auto rr = __builtin_amdgcn_permlane32_swap(__float_as_uint(mx), __float_as_uint(mx), false, false);
          mx = fmaxf(__uint_as_float(rr[0]), __uint_as_float(rr[1])); }
        mx *= CEXP;
        if (__any(mx - m > THR)) {
          const float mn = fmaxf(m, mx), a = __builtin_amdgcn_exp2f(m - mn);
          m = mn; l *= a;
#pragma unroll
          for (int b = 0; b < 6; ++b)
#pragma unroll
            for (int i = 0; i < 16; ++i) o[b][i] *= a;
        }
        float ps = 0.f;
#pragma unroll
        for (int i = 0; i < 16; ++i) { s[i] = __builtin_amdgcn_exp2f(__builtin_fmaf(s[i], CEXP, -m)); ps += s[i]; }
        l += ps;
        u32x4 p0 = {cvtpk(s[0], s[1]), cvtpk(s[2], s[3]), cvtpk(s[4], s[5]), cvtpk(s[6], s[7])};
        u32x4 p1 = {cvtpk(s[8], s[9]), cvtpk(s[10], s[11]), cvtpk(s[12], s[13]), cvtpk(s[14], s[15])};
        const bf16x8 pb0 = __builtin_bit_cast(bf16x8, p0), pb1 = __builtin_bit_cast(bf16x8, p1);
#pragma unroll
        for (int b = 0; b < 6; ++b) {
          const unsigned va = vaddr[b & 1] + so + (b >> 1) * 1024;
          const s16x4 a0 = lds_tr(va), a1 = lds_tr(va + 3072), a2 = lds_tr(va + 6144), a3 = lds_tr(va + 9216);
          o[b] = MFMA(__builtin_shufflevector(a0, a1, 0, 1, 2, 3, 4, 5, 6, 7), pb0, o[b]);
          o[b] = MFMA(__builtin_shufflevector(a2, a3, 0, 1, 2, 3, 4, 5, 6, 7), pb1, o[b]);
        }
      }
    }

    { auto rr = __builtin_amdgcn_permlane32_swap(__float_as_uint(l), __float_as_uint(l), false, false);
      l = __uint_as_float(rr[0]) + __uint_as_float(rr[1]); }
    const int qrow = (qb0 + qi) * 32 + r32;
    const bool ok = act && qrow < L;
    char* rowp = O + (size_t)(s0 - cu0 + min(qrow, L - 1)) * ROWB + head * HEADB + hi * 16;
    if (ks == 1) {
      const float inv = 1.0f / l;
#pragma unroll
      for (int b = 0; b < 6; ++b)
#pragma unroll
        for (int i = 0; i < 16; ++i) o[b][i] *= inv;
      store3(o[0], o[1], o[2], rowp, ok);
      store3(o[3], o[4], o[5], rowp + 192, ok);
      BARRIER();
    } else {
      BARRIER();
      typedef __attribute__((address_space(3))) f32x4* lf4;
      typedef __attribute__((address_space(3))) f32x2* lf2;
      const unsigned xw = lds0 + w * 12800 + lane * 16, xr = lds0 + partner * 12800 + lane * 16;
      if (kh == 0) {
#pragma unroll
        for (int b = 0; b < 3; ++b)
#pragma unroll
          for (int g = 0; g < 4; ++g) *(lf4)(uintptr_t)(xw + (b * 4 + g) * 1024) = f32x4{o[3 + b][4 * g], o[3 + b][4 * g + 1], o[3 + b][4 * g + 2], o[3 + b][4 * g + 3]};
      } else {
#pragma unroll
        for (int b = 0; b < 3; ++b) {
#pragma unroll
          for (int g = 0; g < 4; ++g) *(lf4)(uintptr_t)(xw + (b * 4 + g) * 1024) = f32x4{o[b][4 * g], o[b][4 * g + 1], o[b][4 * g + 2], o[b][4 * g + 3]};
          o[b] = o[3 + b];
        }
      }
      *(lf2)(uintptr_t)(lds0 + w * 12800 + 12288 + lane * 8) = f32x2{m, l};
      BARRIER();
      const f32x2 ml = *(lf2)(uintptr_t)(lds0 + partner * 12800 + 12288 + lane * 8);
      const float mn = fmaxf(m, ml[0]);
      float fa = __builtin_amdgcn_exp2f(m - mn), fb = __builtin_amdgcn_exp2f(ml[0] - mn);
      const float inv = 1.0f / (l * fa + ml[1] * fb);
      fa *= inv; fb *= inv;
#pragma unroll
      for (int b = 0; b < 3; ++b)
#pragma unroll
        for (int g = 0; g < 4; ++g) {
          const f32x4 x = *(lf4)(uintptr_t)(xr + (b * 4 + g) * 1024);
#pragma unroll
          for (int e = 0; e < 4; ++e) o[b][4 * g + e] = __builtin_fmaf(x[e], fb, o[b][4 * g + e] * fa);
        }
      store3(o[0], o[1], o[2], rowp + kh * 192, ok);
      BARRIER();
    }
    item += GPH;
  }
}

extern "C" void kernel_launch(void* const* d_in, const int* in_sizes, int n_in, void* d_out, int out_size, void* d_ws, size_t ws_size,
                              hipStream_t stream) {
  (void)n_in; (void)out_size; (void)d_ws; (void)ws_size;
  const int nseq = in_sizes[3] - 1;
  varlen_attn_kernel<<<NWG, 512, 0, stream>>>((const char*)d_in[0], (const char*)d_in[1], (const char*)d_in[2], (const int*)d_in[3], (char*)d_out, nseq);
}
```

```cpp
#include <hip/hip_runtime.h>
#include <hip/hip_bf16.h>
#include <stdint.h>

typedef short    bf16x8 __attribute__((ext_vector_type(8)));
typedef short    s16x4  __attribute__((ext_vector_type(4)));
typedef float    f32x16 __attribute__((ext_vector_type(16)));
typedef float    f32x4  __attribute__((ext_vector_type(4)));
typedef float    f32x2  __attribute__((ext_vector_type(2)));
typedef __bf16   bf16x2 __attribute__((ext_vector_type(2)));
typedef unsigned u32x4  __attribute__((ext_vector_type(4)));
typedef __attribute__((address_space(3))) char* lds_ptr;

#define DI __device__ __forceinline__
constexpr int H = 16, D = 192, ROWB = H * D * 2, HEADB = D * 2;
constexpr int NWG = 256, GPH = NWG / H;
constexpr int BLKB = 12288;
constexpr int SLOTB = 2 * BLKB, NSLOT = 6;
constexpr int LDSB = 163840;
constexpr float CEXP = 0.07216878364870323f * 1.4426950408889634f;
constexpr float THR = 8.0f;

#define STAMP(k) do {} while (0)
#define MFMA(a, b, c) __builtin_amdgcn_mfma_f32_32x32x16_bf16(a, b, c, 0, 0, 0)

DI unsigned cvtpk(float lo, float hi) { f32x2 v = {lo, hi}; bf16x2 b = __builtin_convertvector(v, bf16x2); return __builtin_bit_cast(unsigned, b); }
DI int rfl(int x) { return __builtin_amdgcn_readfirstlane(x); }

DI void dma3(const void* base, unsigned voff, unsigned ldsb) {
  unsigned keep;
  asm volatile(
      "s_mov_b32 %0, m0\n\t"
      "s_mov_b32 m0, %3\n\t"
      "s_nop 0\n\t"
      "global_load_lds_dwordx4 %1, %2\n\t"
      "s_add_u32 m0, m0, 896\n\t"
      "s_nop 0\n\t"
      "global_load_lds_dwordx4 %1, %2 offset:128\n\t"
      "s_add_u32 m0, m0, 896\n\t"
      "s_nop 0\n\t"
      "global_load_lds_dwordx4 %1, %2 offset:256\n\t"
      "s_mov_b32 m0, %0"
      : "=&s"(keep)
      : "v"(voff), "s"(base), "s"(ldsb)
      : "memory", "scc");
}

DI void wait_vm(int n3) {
  if (n3 <= 0)      asm volatile("s_waitcnt vmcnt(0)" ::: "memory");
  else if (n3 == 1) asm volatile("s_waitcnt vmcnt(3)" ::: "memory");
  else if (n3 == 2) asm volatile("s_waitcnt vmcnt(6)" ::: "memory");
  else if (n3 == 3) asm volatile("s_waitcnt vmcnt(9)" ::: "memory");
  else if (n3 == 4) asm volatile("s_waitcnt vmcnt(12)" ::: "memory");
  else              asm volatile("s_waitcnt vmcnt(15)" ::: "memory");
}
#define BARRIER() asm volatile("s_waitcnt lgkmcnt(0)\n\ts_barrier" ::: "memory")

DI bf16x8 lds_rd128(unsigned a) { return *(const __attribute__((address_space(3))) bf16x8*)(uintptr_t)a; }
DI s16x4 lds_tr(unsigned a) { return __builtin_amdgcn_ds_read_tr16_b64_v4i16((__attribute__((address_space(3))) s16x4*)(uintptr_t)a); }

DI void store3(const f32x16& o0, const f32x16& o1, const f32x16& o2, char* rowp, bool ok) {
#pragma unroll
  for (int b = 0; b < 3; ++b) {
    const f32x16& o = b == 0 ? o0 : (b == 1 ? o1 : o2);
#pragma unroll
    for (int g = 0; g < 4; g += 2) {
      unsigned ax = cvtpk(o[4 * g + 0], o[4 * g + 1]), ay = cvtpk(o[4 * g + 2], o[4 * g + 3]);
      unsigned bx = cvtpk(o[4 * g + 4], o[4 * g + 5]), by = cvtpk(o[4 * g + 6], o[4 * g + 7]);
      auto rx = __builtin_amdgcn_permlane32_swap(ax, bx, false, false);
      auto ry = __builtin_amdgcn_permlane32_swap(ay, by, false, false);
      u32x4 w = {rx[0], ry[0], rx[1], ry[1]};
      if (ok) *(u32x4*)(rowp + b * 64 + g * 16) = w;
    }
  }
}

__global__ __launch_bounds__(512, 2) void varlen_attn_kernel(const char* __restrict__ Q, const char* __restrict__ K, const char* __restrict__ V,
                                                             const int* __restrict__ cu, char* __restrict__ O, int nseq
 ) {
  STAMP(0);
  __shared__ __attribute__((aligned(1024))) char smem[LDSB];
  const unsigned lds0 = (unsigned)(uintptr_t)(lds_ptr)smem;
  const int tid = threadIdx.x, lane = tid & 63, w = rfl(tid >> 6);
  const int r32 = lane & 31, hi = lane >> 5;
  const int bid = blockIdx.x, head = (bid & 7) * 2 + ((bid >> 3) & 1), g0 = bid >> 4;
  const int cu0 = cu[0];

  int thrC;
  {
    int cnt = 0, nmax = 0;
    for (int i0 = 0; i0 < nseq; i0 += 64) { int i = i0 + lane; int n = i < nseq ? (cu[i + 1] - cu[i] + 31) >> 5 : 0; nmax = max(nmax, n); }
#pragma unroll
    for (int off = 32; off >= 1; off >>= 1) nmax = max(nmax, __shfl_xor(nmax, off));
    const int cand = nmax * ((lane >> 3) + 1);
    for (int i0 = 0; i0 < nseq; i0 += 8) {
      int i = i0 + (lane & 7); int n = i < nseq ? (cu[i + 1] - cu[i] + 31) >> 5 : 0;
      int sh = (8 * n <= cand) ? 3 : ((4 * n <= cand) ? 2 : 1);
      cnt += (n + (1 << sh) - 1) >> sh;
    }
    cnt += __shfl_xor(cnt, 1); cnt += __shfl_xor(cnt, 2); cnt += __shfl_xor(cnt, 4);
    unsigned long long okm = __ballot(cnt <= GPH);
    int c = okm ? (__builtin_ctzll(okm) >> 3) : 7;
    thrC = rfl(nmax) * (c + 1);
  }

  const int prow = lane >> 3;
  const int rg = w & 3;
  const unsigned swzK = (unsigned)(((lane & 7) ^ ((4 * rg + (prow >> 1)) & 7)) * 16);
  const unsigned swzV = (unsigned)(((lane & 7) ^ (((prow >> 1) & 1) << 2)) * 16);
  const unsigned swzKV = (w >= 4) ? swzV : swzK;
  unsigned kaddr[4];
#pragma unroll
  for (int s = 0; s < 4; ++s) kaddr[s] = lds0 + (r32 >> 3) * 3072 + (r32 & 7) * 128 + (((2 * s + hi) ^ ((r32 >> 1) & 7)) * 16);
  unsigned vaddr[2];
  { const int q4 = (lane & 15) >> 2, p4 = lane & 3, ch = (lane >> 4) & 1;
    vaddr[0] = lds0 + BLKB + (4 * hi + q4) * 128 + ((q4 >> 1) & 1) * 64 + ch * 32 + p4 * 8; vaddr[1] = vaddr[0] ^ 64; }

  int item = g0, seq = 0, base = 0;
  for (;;) {
    int s0 = 0, L = 0, nkb = 0, sh = 0, cnt = 0;
    for (; seq < nseq; ++seq) {
      s0 = cu[seq]; L = cu[seq + 1] - s0; nkb = (L + 31) >> 5;
      sh = (8 * nkb <= thrC) ? 3 : ((4 * nkb <= thrC) ? 2 : 1);
      cnt = (nkb + (1 << sh) - 1) >> sh;
      if (item < base + cnt) break;
      base += cnt;
    }
    if (seq >= nseq) break;
    const int it = item - base;
    const int qb0 = it << sh, nq = min(1 << sh, nkb - qb0);
    const int ks = nq <= 4 ? 2 : 1;
    int qi, kh, partner; bool act;
    if (ks == 1)      { qi = w; kh = 0; partner = w; act = w < nq; }
    else if (nq <= 2) { qi = w & 1; kh = (w >> 1) & 1; partner = w ^ 2; act = (w < 4) && (qi < nq); }
    else              { qi = w & 3; kh = w >> 2; partner = w ^ 4; act = qi < nq; }
    const int steps = (nkb + ks - 1) / ks, R = NSLOT / ks;
    const int rot = (it * steps) / cnt;
    const int tailn = L - (nkb - 1) * 32;
    const size_t hb = (size_t)s0 * ROWB + (size_t)head * HEADB;
    const char* kvbase = ((w >= 4) ? V : K) + hb;
    const unsigned voffN = (unsigned)(8 * rg + prow) * ROWB + swzKV;
    const unsigned voffT = (unsigned)min(8 * rg + prow, tailn - 1) * ROWB + swzKV;
    const unsigned kvdst = lds0 + ((w >= 4) ? BLKB : 0) + rg * 3072;

    auto issue = [&](int t) {
      int tt = t + rot; if (tt >= steps) tt -= steps;
      for (int j = 0; j < ks; ++j) {
        const int kb = min(tt * ks + j, nkb - 1);
        const int slot = (t * ks + j) % NSLOT;
        dma3(kvbase + (size_t)kb * (32 * ROWB), kb == nkb - 1 ? voffT : voffN, kvdst + slot * SLOTB);
      }
    };

    STAMP(1);
    const unsigned qofs = (ks == 1) ? 2 * SLOTB : 4 * SLOTB;
    for (int g = w; g < nq * 4; g += 8) {
      const int row = min(qb0 * 32 + g * 8 + prow, L - 1);
      dma3(Q + hb, (unsigned)row * ROWB + swzK, lds0 + qofs + g * 3072);
    }
    int issued = min(2, steps);
    for (int t = 0; t < issued; ++t) issue(t);
    wait_vm((issued - 1) * ks);
    BARRIER();
    STAMP(2);
    bf16x8 qf[12];
#pragma unroll
    for (int s = 0; s < 12; ++s) qf[s] = lds_rd128(kaddr[s & 3] + qofs + (act ? qi : 0) * BLKB + (s >> 2) * 1024);
    BARRIER();
    { const int e = min(R, steps); for (; issued < e; ++issued) issue(issued); }

    f32x16 o[6];
#pragma unroll
    for (int b = 0; b < 6; ++b) o[b] = f32x16{};
    float m = -INFINITY, l = 0.f;

    for (int t = 0; t < steps; ++t) {
      if (t == 1) STAMP(3);
      if (t == 2) STAMP(4);
      if (t > 0) {
        wait_vm((issued - 1 - t) * ks);
        BARRIER();
        if (issued < steps) { issue(issued); ++issued; }
      }
      int tt = t + rot; if (tt >= steps) tt -= steps;
      const int kb = tt * ks + kh;
      if (act && kb < nkb) {
        const unsigned so = (unsigned)((t * ks + kh) % NSLOT) * SLOTB;
        f32x16 s = f32x16{};
#pragma unroll
        for (int i = 0; i < 12; ++i) s = MFMA(lds_rd128(kaddr[i & 3] + so + (i >> 2) * 1024), qf[i], s);
        if (kb == nkb - 1 && tailn < 32) {
#pragma unroll
          for (int i = 0; i < 16; ++i) if ((i & 3) + 8 * (i >> 2) + 4 * hi >= tailn) s[i] = -INFINITY;
        }
        float mx = fmaxf(fmaxf(s[0], s[1]), s[2]);
#pragma unroll
        for (int i = 3; i < 15; i += 2) mx = fmaxf(fmaxf(mx, s[i]), s[i + 1]);
        mx = fmaxf(mx, s[15]);
        { auto rr = __builtin_amdgcn_permlane32_swap(__float_as_uint(mx), __float_as_uint(mx), false, false);
          mx = fmaxf(__uint_as_float(rr[0]), __uint_as_float(rr[1])); }
        mx *= CEXP;
        if (__any(mx - m > THR)) {
          const float mn = fmaxf(m, mx), a = __builtin_amdgcn_exp2f(m - mn);
          m = mn; l *= a;
#pragma unroll
          for (int b = 0; b < 6; ++b)
#pragma unroll
            for (int i = 0; i < 16; ++i) o[b][i] *= a;
        }
        float ps = 0.f;
#pragma unroll
        for (int i = 0; i < 16; ++i) { s[i] = __builtin_amdgcn_exp2f(__builtin_fmaf(s[i], CEXP, -m)); ps += s[i]; }
        l += ps;
        u32x4 p0 = {cvtpk(s[0], s[1]), cvtpk(s[2], s[3]), cvtpk(s[4], s[5]), cvtpk(s[6], s[7])};
        u32x4 p1 = {cvtpk(s[8], s[9]), cvtpk(s[10], s[11]), cvtpk(s[12], s[13]), cvtpk(s[14], s[15])};
        const bf16x8 pb0 = __builtin_bit_cast(bf16x8, p0), pb1 = __builtin_bit_cast(bf16x8, p1);
#pragma unroll
        for (int b = 0; b < 6; ++b) {
          const unsigned va = vaddr[b & 1] + so + (b >> 1) * 1024;
          const s16x4 a0 = lds_tr(va), a1 = lds_tr(va + 3072), a2 = lds_tr(va + 6144), a3 = lds_tr(va + 9216);
          o[b] = MFMA(__builtin_shufflevector(a0, a1, 0, 1, 2, 3, 4, 5, 6, 7), pb0, o[b]);
          o[b] = MFMA(__builtin_shufflevector(a2, a3, 0, 1, 2, 3, 4, 5, 6, 7), pb1, o[b]);
        }
      }
    }

    STAMP(5);
    { auto rr = __builtin_amdgcn_permlane32_swap(__float_as_uint(l), __float_as_uint(l), false, false);
      l = __uint_as_float(rr[0]) + __uint_as_float(rr[1]); }
    const int qrow = (qb0 + qi) * 32 + r32;
    const bool ok = act && qrow < L;
    char* rowp = O + (size_t)(s0 - cu0 + min(qrow, L - 1)) * ROWB + head * HEADB + hi * 16;
    if (ks == 1) {
      const float inv = 1.0f / l;
#pragma unroll
      for (int b = 0; b < 6; ++b)
#pragma unroll
        for (int i = 0; i < 16; ++i) o[b][i] *= inv;
      store3(o[0], o[1], o[2], rowp, ok);
      store3(o[3], o[4], o[5], rowp + 192, ok);
      BARRIER();
    } else {
      BARRIER();
      typedef __attribute__((address_space(3))) f32x4* lf4;
      typedef __attribute__((address_space(3))) f32x2* lf2;
      const unsigned xw = lds0 + w * 12800 + lane * 16, xr = lds0 + partner * 12800 + lane * 16;
      if (kh == 0) {
#pragma unroll
        for (int b = 0; b < 3; ++b)
#pragma unroll
          for (int g = 0; g < 4; ++g) *(lf4)(uintptr_t)(xw + (b * 4 + g) * 1024) = f32x4{o[3 + b][4 * g], o[3 + b][4 * g + 1], o[3 + b][4 * g + 2], o[3 + b][4 * g + 3]};
      } else {
#pragma unroll
        for (int b = 0; b < 3; ++b) {
#pragma unroll
          for (int g = 0; g < 4; ++g) *(lf4)(uintptr_t)(xw + (b * 4 + g) * 1024) = f32x4{o[b][4 * g], o[b][4 * g + 1], o[b][4 * g + 2], o[b][4 * g + 3]};
          o[b] = o[3 + b];
        }
      }
      *(lf2)(uintptr_t)(lds0 + w * 12800 + 12288 + lane * 8) = f32x2{m, l};
      BARRIER();
      const f32x2 ml = *(lf2)(uintptr_t)(lds0 + partner * 12800 + 12288 + lane * 8);
      const float mn = fmaxf(m, ml[0]);
      float fa = __builtin_amdgcn_exp2f(m - mn), fb = __builtin_amdgcn_exp2f(ml[0] - mn);
      const float inv = 1.0f / (l * fa + ml[1] * fb);
      fa *= inv; fb *= inv;
#pragma unroll
      for (int b = 0; b < 3; ++b)
#pragma unroll
        for (int g = 0; g < 4; ++g) {
          const f32x4 x = *(lf4)(uintptr_t)(xr + (b * 4 + g) * 1024);
#pragma unroll
          for (int e = 0; e < 4; ++e) o[b][4 * g + e] = __builtin_fmaf(x[e], fb, o[b][4 * g + e] * fa);
        }
      store3(o[0], o[1], o[2], rowp + kh * 192, ok);
      BARRIER();
    }
    STAMP(6);
    item += GPH;
  }
}

extern "C" void kernel_launch(void* const* d_in, const int* in_sizes, int n_in, void* d_out, int out_size, void* d_ws, size_t ws_size,
                              hipStream_t stream) {
  (void)n_in; (void)out_size; (void)d_ws; (void)ws_size;
  const int nseq = in_sizes[3] - 1;
  varlen_attn_kernel<<<NWG, 512, 0, stream>>>((const char*)d_in[0], (const char*)d_in[1], (const char*)d_in[2], (const int*)d_in[3], (char*)d_out, nseq
  );
}
```

```cpp
#define STAMPS
#define PROBE_BID 96
#define PROBE_K 0
#include <hip/hip_runtime.h>
#include <hip/hip_bf16.h>
#include <stdint.h>

typedef short    bf16x8 __attribute__((ext_vector_type(8)));
typedef short    s16x4  __attribute__((ext_vector_type(4)));
typedef float    f32x16 __attribute__((ext_vector_type(16)));
typedef float    f32x4  __attribute__((ext_vector_type(4)));
typedef float    f32x2  __attribute__((ext_vector_type(2)));
typedef __bf16   bf16x2 __attribute__((ext_vector_type(2)));
typedef unsigned u32x4  __attribute__((ext_vector_type(4)));
typedef __attribute__((address_space(3))) char* lds_ptr;

#define DI __device__ __forceinline__
constexpr int H = 16, D = 192, ROWB = H * D * 2, HEADB = D * 2;
constexpr int NWG = 256, GPH = NWG / H;
constexpr int BLKB = 12288;
constexpr int SLOTB = 2 * BLKB, NSLOT = 6;
constexpr int LDSB = 163840;
constexpr float CEXP = 0.07216878364870323f * 1.4426950408889634f;
constexpr float THR = 8.0f;

#define NSTAMP 9
#define STAMP(k) do { __builtin_amdgcn_sched_barrier(0); asm volatile("s_memrealtime %0\n\ts_waitcnt lgkmcnt(0)" : "=s"(st_[k]) :: "memory"); __builtin_amdgcn_sched_barrier(0); } while (0)
#define MFMA(a, b, c) __builtin_amdgcn_mfma_f32_32x32x16_bf16(a, b, c, 0, 0, 0)

DI unsigned cvtpk(float lo, float hi) { f32x2 v = {lo, hi}; bf16x2 b = __builtin_convertvector(v, bf16x2); return __builtin_bit_cast(unsigned, b); }
DI int rfl(int x) { return __builtin_amdgcn_readfirstlane(x); }

DI void dma3(const void* base, unsigned voff, unsigned ldsb) {
  unsigned keep;
  asm volatile(
      "s_mov_b32 %0, m0\n\t"
      "s_mov_b32 m0, %3\n\t"
      "s_nop 0\n\t"
      "global_load_lds_dwordx4 %1, %2\n\t"
      "s_add_u32 m0, m0, 896\n\t"
      "s_nop 0\n\t"
      "global_load_lds_dwordx4 %1, %2 offset:128\n\t"
      "s_add_u32 m0, m0, 896\n\t"
      "s_nop 0\n\t"
      "global_load_lds_dwordx4 %1, %2 offset:256\n\t"
      "s_mov_b32 m0, %0"
      : "=&s"(keep)
      : "v"(voff), "s"(base), "s"(ldsb)
      : "memory", "scc");
}

DI void wait_vm(int n3) {
  if (n3 <= 0)      asm volatile("s_waitcnt vmcnt(0)" ::: "memory");
  else if (n3 == 1) asm volatile("s_waitcnt vmcnt(3)" ::: "memory");
  else if (n3 == 2) asm volatile("s_waitcnt vmcnt(6)" ::: "memory");
  else if (n3 == 3) asm volatile("s_waitcnt vmcnt(9)" ::: "memory");
  else if (n3 == 4) asm volatile("s_waitcnt vmcnt(12)" ::: "memory");
  else              asm volatile("s_waitcnt vmcnt(15)" ::: "memory");
}
#define BARRIER() asm volatile("s_waitcnt lgkmcnt(0)\n\ts_barrier" ::: "memory")

DI bf16x8 lds_rd128(unsigned a) { return *(const __attribute__((address_space(3))) bf16x8*)(uintptr_t)a; }
DI s16x4 lds_tr(unsigned a) { return __builtin_amdgcn_ds_read_tr16_b64_v4i16((__attribute__((address_space(3))) s16x4*)(uintptr_t)a); }

DI void store3(const f32x16& o0, const f32x16& o1, const f32x16& o2, char* rowp, bool ok) {
#pragma unroll
  for (int b = 0; b < 3; ++b) {
    const f32x16& o = b == 0 ? o0 : (b == 1 ? o1 : o2);
#pragma unroll
    for (int g = 0; g < 4; g += 2) {
      unsigned ax = cvtpk(o[4 * g + 0], o[4 * g + 1]), ay = cvtpk(o[4 * g + 2], o[4 * g + 3]);
      unsigned bx = cvtpk(o[4 * g + 4], o[4 * g + 5]), by = cvtpk(o[4 * g + 6], o[4 * g + 7]);
      auto rx = __builtin_amdgcn_permlane32_swap(ax, bx, false, false);
      auto ry = __builtin_amdgcn_permlane32_swap(ay, by, false, false);
      u32x4 w = {rx[0], ry[0], rx[1], ry[1]};
      if (ok) asm volatile("global_store_dwordx4 %0, %1, off sc0 sc1\n\ts_nop 1" :: "v"(rowp + b * 64 + g * 16), "v"(w) : "memory");
    }
  }
}

__global__ __launch_bounds__(512, 2) void varlen_attn_kernel(const char* __restrict__ Q, const char* __restrict__ K, const char* __restrict__ V,
                                                             const int* __restrict__ cu, char* __restrict__ O, int nseq
 ) {
  unsigned long long st_[NSTAMP] = {}; unsigned long long c0_ = __builtin_readcyclecounter();
  STAMP(0);
  __shared__ __attribute__((aligned(1024))) char smem[LDSB];
  const unsigned lds0 = (unsigned)(uintptr_t)(lds_ptr)smem;
  const int tid = threadIdx.x, lane = tid & 63, w = rfl(tid >> 6);
  const int r32 = lane & 31, hi = lane >> 5;
  const int bid = blockIdx.x, head = (bid & 7) * 2 + ((bid >> 3) & 1), g0 = bid >> 4;
  const int cu0 = cu[0];

  int thrC;
  {
    int cnt = 0, nmax = 0;
    for (int i0 = 0; i0 < nseq; i0 += 64) { int i = i0 + lane; int n = i < nseq ? (cu[i + 1] - cu[i] + 31) >> 5 : 0; nmax = max(nmax, n); }
#pragma unroll
    for (int off = 32; off >= 1; off >>= 1) nmax = max(nmax, __shfl_xor(nmax, off));
    const int cand = nmax * ((lane >> 3) + 1);
    for (int i0 = 0; i0 < nseq; i0 += 8) {
      int i = i0 + (lane & 7); int n = i < nseq ? (cu[i + 1] - cu[i] + 31) >> 5 : 0;
      int sh = (8 * n <= cand) ? 3 : ((4 * n <= cand) ? 2 : 1);
      cnt += (n + (1 << sh) - 1) >> sh;
    }
    cnt += __shfl_xor(cnt, 1); cnt += __shfl_xor(cnt, 2); cnt += __shfl_xor(cnt, 4);
    unsigned long long okm = __ballot(cnt <= GPH);
    int c = okm ? (__builtin_ctzll(okm) >> 3) : 7;
    thrC = rfl(nmax) * (c + 1);
  }

  const int prow = lane >> 3;
  const int rg = w & 3;
  const unsigned swzK = (unsigned)(((lane & 7) ^ ((4 * rg + (prow >> 1)) & 7)) * 16);
  const unsigned swzV = (unsigned)(((lane & 7) ^ (((prow >> 1) & 1) << 2)) * 16);
  const unsigned swzKV = (w >= 4) ? swzV : swzK;
  unsigned kaddr[4];
#pragma unroll
  for (int s = 0; s < 4; ++s) kaddr[s] = lds0 + (r32 >> 3) * 3072 + (r32 & 7) * 128 + (((2 * s + hi) ^ ((r32 >> 1) & 7)) * 16);
  unsigned vaddr[2];
  { const int q4 = (lane & 15) >> 2, p4 = lane & 3, ch = (lane >> 4) & 1;
    vaddr[0] = lds0 + BLKB + (4 * hi + q4) * 128 + ((q4 >> 1) & 1) * 64 + ch * 32 + p4 * 8; vaddr[1] = vaddr[0] ^ 64; }

  int item = g0, seq = 0, base = 0;
  for (;;) {
    int s0 = 0, L = 0, nkb = 0, sh = 0, cnt = 0;
    for (; seq < nseq; ++seq) {
      s0 = cu[seq]; L = cu[seq + 1] - s0; nkb = (L + 31) >> 5;
      sh = (8 * nkb <= thrC) ? 3 : ((4 * nkb <= thrC) ? 2 : 1);
      cnt = (nkb + (1 << sh) - 1) >> sh;
      if (item < base + cnt) break;
      base += cnt;
    }
    if (seq >= nseq) break;
    const int it = item - base;
    const int qb0 = it << sh, nq = min(1 << sh, nkb - qb0);
    const int ks = nq <= 4 ? 2 : 1;
    int qi, kh, partner; bool act;
    if (ks == 1)      { qi = w; kh = 0; partner = w; act = w < nq; }
    else if (nq <= 2) { qi = w & 1; kh = (w >> 1) & 1; partner = w ^ 2; act = (w < 4) && (qi < nq); }
    else              { qi = w & 3; kh = w >> 2; partner = w ^ 4; act = qi < nq; }
    const int steps = (nkb + ks - 1) / ks, R = NSLOT / ks;
    const int rot = (it * steps) / cnt;
    const int tailn = L - (nkb - 1) * 32;
    const size_t hb = (size_t)s0 * ROWB + (size_t)head * HEADB;
    const char* kvbase = ((w >= 4) ? V : K) + hb;
    const unsigned voffN = (unsigned)(8 * rg + prow) * ROWB + swzKV;
    const unsigned voffT = (unsigned)min(8 * rg + prow, tailn - 1) * ROWB + swzKV;
    const unsigned kvdst = lds0 + ((w >= 4) ? BLKB : 0) + rg * 3072;

    auto issue = [&](int t) {
      int tt = t + rot; if (tt >= steps) tt -= steps;
      for (int j = 0; j < ks; ++j) {
        const int kb = min(tt * ks + j, nkb - 1);
        const int slot = (t * ks + j) % NSLOT;
        dma3(kvbase + (size_t)kb * (32 * ROWB), kb == nkb - 1 ? voffT : voffN, kvdst + slot * SLOTB);
      }
    };

    STAMP(1);
    const unsigned qofs = (ks == 1) ? 2 * SLOTB : 4 * SLOTB;
    for (int g = w; g < nq * 4; g += 8) {
      const int row = min(qb0 * 32 + g * 8 + prow, L - 1);
      dma3(Q + hb, (unsigned)row * ROWB + swzK, lds0 + qofs + g * 3072);
    }
    int issued = min(2, steps);
    for (int t = 0; t < issued; ++t) issue(t);
    wait_vm((issued - 1) * ks);
    BARRIER();
    STAMP(2);
    bf16x8 qf[12];
#pragma unroll
    for (int s = 0; s < 12; ++s) qf[s] = lds_rd128(kaddr[s & 3] + qofs + (act ? qi : 0) * BLKB + (s >> 2) * 1024);
    BARRIER();
    { const int e = min(R, steps); for (; issued < e; ++issued) issue(issued); }

    f32x16 o[6];
#pragma unroll
    for (int b = 0; b < 6; ++b) o[b] = f32x16{};
    float m = -INFINITY, l = 0.f;

    for (int t = 0; t < steps; ++t) {
      if (t == 1) STAMP(3);
      if (t == 2) STAMP(4);
      if (t > 0) {
        wait_vm((issued - 1 - t) * ks);
        BARRIER();
        if (issued < steps) { issue(issued); ++issued; }
      }
      int tt = t + rot; if (tt >= steps) tt -= steps;
      const int kb = tt * ks + kh;
      if (act && kb < nkb) {
        const unsigned so = (unsigned)((t * ks + kh) % NSLOT) * SLOTB;
        f32x16 s = f32x16{};
#pragma unroll
        for (int i = 0; i < 12; ++i) s = MFMA(lds_rd128(kaddr[i & 3] + so + (i >> 2) * 1024), qf[i], s);
        if (kb == nkb - 1 && tailn < 32) {
#pragma unroll
          for (int i = 0; i < 16; ++i) if ((i & 3) + 8 * (i >> 2) + 4 * hi >= tailn) s[i] = -INFINITY;
        }
        float mx = fmaxf(fmaxf(s[0], s[1]), s[2]);
#pragma unroll
        for (int i = 3; i < 15; i += 2) mx = fmaxf(fmaxf(mx, s[i]), s[i + 1]);
        mx = fmaxf(mx, s[15]);
        { auto rr = __builtin_amdgcn_permlane32_swap(__float_as_uint(mx), __float_as_uint(mx), false, false);
          mx = fmaxf(__uint_as_float(rr[0]), __uint_as_float(rr[1])); }
        mx *= CEXP;
        if (__any(mx - m > THR)) {
          const float mn = fmaxf(m, mx), a = __builtin_amdgcn_exp2f(m - mn);
          m = mn; l *= a;
#pragma unroll
          for (int b = 0; b < 6; ++b)
#pragma unroll
            for (int i = 0; i < 16; ++i) o[b][i] *= a;
        }
        float ps = 0.f;
#pragma unroll
        for (int i = 0; i < 16; ++i) { s[i] = __builtin_amdgcn_exp2f(__builtin_fmaf(s[i], CEXP, -m)); ps += s[i]; }
        l += ps;
        u32x4 p0 = {cvtpk(s[0], s[1]), cvtpk(s[2], s[3]), cvtpk(s[4], s[5]), cvtpk(s[6], s[7])};
        u32x4 p1 = {cvtpk(s[8], s[9]), cvtpk(s[10], s[11]), cvtpk(s[12], s[13]), cvtpk(s[14], s[15])};
        const bf16x8 pb0 = __builtin_bit_cast(bf16x8, p0), pb1 = __builtin_bit_cast(bf16x8, p1);
#pragma unroll
        for (int b = 0; b < 6; ++b) {
          const unsigned va = vaddr[b & 1] + so + (b >> 1) * 1024;
          const s16x4 a0 = lds_tr(va), a1 = lds_tr(va + 3072), a2 = lds_tr(va + 6144), a3 = lds_tr(va + 9216);
          o[b] = MFMA(__builtin_shufflevector(a0, a1, 0, 1, 2, 3, 4, 5, 6, 7), pb0, o[b]);
          o[b] = MFMA(__builtin_shufflevector(a2, a3, 0, 1, 2, 3, 4, 5, 6, 7), pb1, o[b]);
        }
      }
    }

    STAMP(5);
    { auto rr = __builtin_amdgcn_permlane32_swap(__float_as_uint(l), __float_as_uint(l), false, false);
      l = __uint_as_float(rr[0]) + __uint_as_float(rr[1]); }
    const int qrow = (qb0 + qi) * 32 + r32;
    const bool ok = act && qrow < L;
    char* rowp = O + (size_t)(s0 - cu0 + min(qrow, L - 1)) * ROWB + head * HEADB + hi * 16;
    if (ks == 1) {
      const float inv = 1.0f / l;
#pragma unroll
      for (int b = 0; b < 6; ++b)
#pragma unroll
        for (int i = 0; i < 16; ++i) o[b][i] *= inv;
      store3(o[0], o[1], o[2], rowp, ok);
      store3(o[3], o[4], o[5], rowp + 192, ok);
      BARRIER();
    } else {
      BARRIER();
      typedef __attribute__((address_space(3))) f32x4* lf4;
      typedef __attribute__((address_space(3))) f32x2* lf2;
      const unsigned xw = lds0 + w * 12800 + lane * 16, xr = lds0 + partner * 12800 + lane * 16;
      if (kh == 0) {
#pragma unroll
        for (int b = 0; b < 3; ++b)
#pragma unroll
          for (int g = 0; g < 4; ++g) *(lf4)(uintptr_t)(xw + (b * 4 + g) * 1024) = f32x4{o[3 + b][4 * g], o[3 + b][4 * g + 1], o[3 + b][4 * g + 2], o[3 + b][4 * g + 3]};
      } else {
#pragma unroll
        for (int b = 0; b < 3; ++b) {
#pragma unroll
          for (int g = 0; g < 4; ++g) *(lf4)(uintptr_t)(xw + (b * 4 + g) * 1024) = f32x4{o[b][4 * g], o[b][4 * g + 1], o[b][4 * g + 2], o[b][4 * g + 3]};
          o[b] = o[3 + b];
        }
      }
      *(lf2)(uintptr_t)(lds0 + w * 12800 + 12288 + lane * 8) = f32x2{m, l};
      BARRIER();
      const f32x2 ml = *(lf2)(uintptr_t)(lds0 + partner * 12800 + 12288 + lane * 8);
      const float mn = fmaxf(m, ml[0]);
      float fa = __builtin_amdgcn_exp2f(m - mn), fb = __builtin_amdgcn_exp2f(ml[0] - mn);
      const float inv = 1.0f / (l * fa + ml[1] * fb);
      fa *= inv; fb *= inv;
#pragma unroll
      for (int b = 0; b < 3; ++b)
#pragma unroll
        for (int g = 0; g < 4; ++g) {
          const f32x4 x = *(lf4)(uintptr_t)(xr + (b * 4 + g) * 1024);
#pragma unroll
          for (int e = 0; e < 4; ++e) o[b][4 * g + e] = __builtin_fmaf(x[e], fb, o[b][4 * g + e] * fa);
        }
      store3(o[0], o[1], o[2], rowp + kh * 192, ok);
      BARRIER();
    }
    STAMP(6);
    item += GPH;
  }
  asm volatile("s_waitcnt vmcnt(0)" ::: "memory");
  STAMP(7);
  st_[NSTAMP - 1] = __builtin_readcyclecounter() - c0_;
  if (bid == PROBE_BID) { const unsigned long long tgt = st_[0] + 6000 + 10 * (st_[PROBE_K] - st_[0]); for (int it = 0; it < 2000000; ++it) { if (__builtin_amdgcn_s_memrealtime() >= tgt) break; __builtin_amdgcn_s_sleep(2); } }
}

extern "C" void kernel_launch(void* const* d_in, const int* in_sizes, int n_in, void* d_out, int out_size, void* d_ws, size_t ws_size,
                              hipStream_t stream) {
  (void)n_in; (void)out_size; (void)d_ws; (void)ws_size;
  const int nseq = in_sizes[3] - 1;
  varlen_attn_kernel<<<NWG, 512, 0, stream>>>((const char*)d_in[0], (const char*)d_in[1], (const char*)d_in[2], (const int*)d_in[3], (char*)d_out, nseq
  );
}
```

```cpp
#include <hip/hip_runtime.h>
#include <hip/hip_bf16.h>
#include <stdint.h>

typedef short    bf16x8 __attribute__((ext_vector_type(8)));
typedef short    s16x4  __attribute__((ext_vector_type(4)));
typedef float    f32x16 __attribute__((ext_vector_type(16)));
typedef float    f32x4  __attribute__((ext_vector_type(4)));
typedef float    f32x2  __attribute__((ext_vector_type(2)));
typedef __bf16   bf16x2 __attribute__((ext_vector_type(2)));
typedef unsigned u32x4  __attribute__((ext_vector_type(4)));
typedef unsigned u32x2  __attribute__((ext_vector_type(2)));
typedef unsigned u32x8  __attribute__((ext_vector_type(8)));
typedef unsigned u32x16 __attribute__((ext_vector_type(16)));
typedef __attribute__((address_space(3))) char* lds_ptr;

#define DI __device__ __forceinline__
constexpr int H = 16, D = 192, ROWB = H * D * 2, HEADB = D * 2;
constexpr int NWG = 256, GPH = NWG / H;
constexpr int BLKB = 12288;
constexpr int KOFS = BLKB, VOFS = 7 * BLKB, STOFS = 13 * BLKB, LDSB = 163840;
constexpr float CEXP = 0.07216878364870323f * 1.4426950408889634f;
constexpr float THR = 8.0f;

#define STAUX 16
#define PFMODE 1
#define CODEB 15000
#define THR1 -1
#define THR2 -1
#define ROT 0
#define PFH 1
#define HDLY 2
#define OFFL 1
#define SELFPF 0
#define HB 0
#define HN 2
#define DBL 1
#define PFL 8
#define PFP 0
#define PFC 1
#define HS 0
#define ORD 1
#define DELAY 22
#define MFMA(a, b, c) __builtin_amdgcn_mfma_f32_32x32x16_bf16(a, b, c, 0, 0, 0)
#define SBAR() __builtin_amdgcn_sched_barrier(0)
#define PIN(x) asm volatile("" : "+v"(x))
#define PINS(x) asm volatile("" : "+s"(x))
#define LIKELY(x) __builtin_expect(!!(x), 1)
#define UNLIKELY(x) __builtin_expect(!!(x), 0)

DI unsigned cvtpk(float lo, float hi) { f32x2 v = {lo, hi}; bf16x2 b = __builtin_convertvector(v, bf16x2); return __builtin_bit_cast(unsigned, b); }
DI int rfl(int x) { return __builtin_amdgcn_readfirstlane(x); }
DI int rdl(int x, int l) { return __builtin_amdgcn_readlane(x, l); }
template <int CTRL> DI int dpp0(int x) { return __builtin_amdgcn_update_dpp(0, x, CTRL, 0xf, 0xf, true); }

typedef __amdgpu_buffer_rsrc_t srd_t;
DI void dma3(srd_t srd, unsigned voff, unsigned soff, unsigned ldsb) {
  unsigned keep;
  asm volatile(
      "s_mov_b32 %0, m0\n\t"
      "s_mov_b32 m0, %4\n\t"
      "s_nop 2\n\t"
      "buffer_load_dwordx4 %1, %2, %3 offen lds\n\t"
      "s_add_u32 m0, m0, 896\n\t"
      "s_nop 0\n\t"
      "buffer_load_dwordx4 %1, %2, %3 offen offset:128 lds\n\t"
      "s_add_u32 m0, m0, 896\n\t"
      "s_nop 0\n\t"
      "buffer_load_dwordx4 %1, %2, %3 offen offset:256 lds\n\t"
      "s_mov_b32 m0, %0"
      : "=&s"(keep)
      : "v"(voff), "s"(srd), "s"(soff), "s"(ldsb)
      : "memory", "scc");
}
#define BARRIER() asm volatile("s_waitcnt lgkmcnt(0)\n\ts_barrier" ::: "memory")

DI bf16x8 lds_rd128(unsigned a) { return *(const __attribute__((address_space(3))) bf16x8*)(uintptr_t)a; }
DI s16x4 lds_tr(unsigned a) { return __builtin_amdgcn_ds_read_tr16_b64_v4i16((__attribute__((address_space(3))) s16x4*)(uintptr_t)a); }
DI bf16x8 vfrag(unsigned a) { const s16x4 x = lds_tr(a), y = lds_tr(a + 3072); return __builtin_shufflevector(x, y, 0, 1, 2, 3, 4, 5, 6, 7); }
typedef __attribute__((address_space(3))) f32x4* lf4;
typedef __attribute__((address_space(3))) f32x2* lf2;
typedef __attribute__((address_space(3))) u32x2* lu2;
typedef __attribute__((address_space(3))) u32x4* lu4;
DI int opaque_tid() { int t = threadIdx.x; asm volatile("" : "+v"(t)); return t; }

__global__ __launch_bounds__(512, 2) void varlen_attn_kernel(const char* __restrict__ Q, const char* __restrict__ K, const char* __restrict__ V,
                                                             const int* __restrict__ cu, char* __restrict__ O, int nseq, int T2
 ) {
  asm volatile("" :: "s"(Q), "s"(K), "s"(V), "s"(O), "s"(cu), "s"(nseq), "s"(T2));
  __shared__ __attribute__((aligned(1024))) char smem[LDSB];
  const unsigned lds0 = (unsigned)(uintptr_t)(lds_ptr)smem;
  const int tid = threadIdx.x, lane = tid & 63, w = rfl(tid >> 6);
  const int bid = blockIdx.x, head = (bid & 7) * 2 + ((bid >> 3) & 1), item = bid >> 4;

  const int sq = lane & 7, prow = lane >> 3, rg = w & 3;
  const int sqc = min(sq, nseq - 1);
  int a0 = cu[sqc], a1 = cu[sqc + 1];
  unsigned pf0 = 0, pf1 = 0;
  unsigned ka0, ka1, ka2, ka3, va0;
  { const int r32 = lane & 31, hi = lane >> 5, x = (r32 >> 1) & 7; const unsigned b = lds0 + (r32 >> 3) * 3072 + (r32 & 7) * 128;
    ka0 = b + ((hi ^ x) * 16); ka1 = b + (((2 + hi) ^ x) * 16); ka2 = b + (((4 + hi) ^ x) * 16); ka3 = b + (((6 + hi) ^ x) * 16);
    const int q4 = (lane & 15) >> 2, p4 = lane & 3, ch = (lane >> 4) & 1;
    va0 = lds0 + VOFS + (4 * hi + q4) * 128 + ((q4 >> 1) & 1) * 64 + ch * 32 + p4 * 8; }
  unsigned kvo = (unsigned)(8 * rg + prow) * ROWB + (unsigned)((sq ^ ((4 * rg + (prow >> 1)) & 7)) * 16);
  unsigned vvo = (unsigned)(8 * rg + prow) * ROWB + (unsigned)((sq ^ (((prow >> 1) & 1) << 2)) * 16);
  PIN(ka0); PIN(ka1); PIN(ka2); PIN(ka3); PIN(va0); PIN(kvo); PIN(vvo);
  int mk1 = sq >= 1 ? -1 : 0, mk2 = sq >= 2 ? -1 : 0, mk4 = sq >= 4 ? -1 : 0, mval = sq < nseq ? -1 : 0, minf = prow == 7 ? 0x3fffffff : 0;
  Q += head * HEADB; K += head * HEADB; V += head * HEADB; O += head * HEADB;
  asm volatile("" :: "s"(Q), "s"(K), "s"(V), "s"(O), "s"(item));
  PIN(mk1); PIN(mk2); PIN(mk4); PIN(mval); PIN(minf);
  PIN(a0); PIN(a1);
  { unsigned long long pc;
    asm volatile("s_cmp_lt_u32 %3, 6\n\ts_cbranch_scc1 1f\n\ts_getpc_b64 %1\n\tglobal_load_dword %0, %2, %1\n1:" : "+{v240}"(pf0), "=&s"(pc) : "v"(min(((w & 1) * 64 + lane) * 128, CODEB)), "s"(w) : "memory", "scc"); }

  int s0, L, qb0, nq, slack = 0, lbase = 0, lln = 0, lit = 0, vpc = 0, vsl = 0;
  const int cu0 = rdl(a0, 0);
  if (LIKELY(nseq <= 8)) {
    const int n = ((a1 - a0 + 31) >> 5) & mval, c2 = (n + 1) >> 1, wt = c2 + 2;
    int tot = __mul24(n, wt), wmx = wt;
    tot += dpp0<0xB1>(tot); wmx = max(wmx, dpp0<0xB1>(wmx));
    tot += dpp0<0x4E>(tot); wmx = max(wmx, dpp0<0x4E>(wmx));
    tot += dpp0<0x141>(tot); wmx = max(wmx, dpp0<0x141>(wmx));
    const float fn = (float)n;
    const int c8 = (n + 7) >> 3, c6 = (int)((fn + 5.5f) * (1.0f / 6.0f)), c4 = (n + 3) >> 2;
    const int lb = max(wmx, (int)((unsigned)(tot + 2 * GPH - 1) / (2u * GPH)));
    const int C = (lb + __mul24(prow, (lb + 15) >> 4)) | minf;
    int pc = C >= 2 * wt ? c4 : c2; pc = C >= 3 * wt ? c6 : pc; pc = C >= 4 * wt ? c8 : pc;
    int qd = (int)((fn + 0.5f) * __builtin_amdgcn_rcpf((float)pc)), rem = n - __mul24(qd, pc);
    int sl = C - __mul24((qd + (rem > 0 ? 2 : 1)) >> 1, wt) - 2;
    int ppre = sl <= 0 ? pc : pc << 16;
    ppre += dpp0<0x111>(ppre) & mk1;
    ppre += dpp0<0x112>(ppre) & mk2;
    ppre += dpp0<0x114>(ppre) & mk4;
    asm volatile("" : "+v"(ppre), "+v"(qd), "+v"(rem), "+v"(sl));
    const unsigned long long okm = (__ballot((ppre & 0xffff) + (ppre >> 16) <= GPH) & 0x8080808080808080ull) | 0x8000000000000000ull;
    const int pbase = __builtin_ctzll(okm) - 7;
    const int ncr = ORD ? rdl(ppre, pbase + 7) & 0xffff : 0x7fff;
    const int ix = item < ncr ? item : item - ncr;
    ppre = item < ncr ? ppre & 0xffff : ppre >> 16;
    const unsigned m8 = (unsigned)(__ballot(ppre > ix) >> pbase) & 0xffu;
    if (m8 == 0) return;
    const int ln = pbase + __builtin_ctz(m8);
    s0 = rdl(a0, ln); L = rdl(a1, ln) - s0;
    const int it = ix - rdl(ppre - pc, ln), sqd = rdl(qd, ln), srem = rdl(rem, ln);
    qb0 = it * sqd + min(it, srem); nq = sqd + (it < srem ? 1 : 0);
    slack = rdl(sl & ~minf, ln);
    lbase = pbase; lln = ln; lit = it; vpc = pc; vsl = sl;
  } else {
    s0 = 0; L = 0; int cnt = 0, gseq = 0, gbase = 0;
    for (; gseq < nseq; ++gseq) {
      s0 = cu[gseq]; L = cu[gseq + 1] - s0; cnt = (L + 127) >> 7;
      if (item < gbase + cnt) break;
      gbase += cnt;
    }
    if (gseq >= nseq) return;
    qb0 = (item - gbase) * 4; nq = min(4, ((L + 31) >> 5) - qb0);
  }
  const int nkb = (L + 31) >> 5, S = (nkb + 1) >> 1;
  const size_t hb = (size_t)s0 * ROWB;
  const int seqb = (L - 1) * ROWB + HEADB;
  const srd_t ksrd = __builtin_amdgcn_make_buffer_rsrc((void*)(K + hb), 0, seqb, 0x00020000);
  const srd_t vsrd = __builtin_amdgcn_make_buffer_rsrc((void*)(V + hb), 0, seqb, 0x00020000);
  const srd_t qsrd = __builtin_amdgcn_make_buffer_rsrc((void*)(Q + hb), 0, seqb, 0x00020000);
  if (UNLIKELY(DELAY && slack > 0)) {
    const unsigned long long tend = __builtin_amdgcn_s_memrealtime() + (unsigned)(min(slack, 7) * DELAY);
    const unsigned cm = (unsigned)(__ballot(vpc >= PFC && vsl <= 0) >> lbase) & 0xffu;
    int hp = vsl >= 5 ? vpc : 0;
    hp += dpp0<0x111>(hp) & mk1; hp += dpp0<0x112>(hp) & mk2; hp += dpp0<0x114>(hp) & mk4;
    if (PFH && cm != 0 && slack >= 5) {
      const int hj = rdl(hp - vpc, lln) + lit, hn = rdl(hp, lbase + 7);
      const int nc2 = 2 * __builtin_popcount(cm), id = w * hn + hj;
      const int ts = rfl((int)(((float)id + 0.5f) * __builtin_amdgcn_rcpf((float)nc2)));
      int r = id - ts * nc2 + 2 * ts; while (r >= nc2) r -= nc2;
      for (int i = min(ts, 7); i > 0; --i) __builtin_amdgcn_s_sleep(HDLY);
      unsigned m = cm; for (int i = r >> 1; i > 0; --i) m &= m - 1;
      const int lc = lbase + __builtin_ctz(m), c0 = rdl(a0, lc), cl = rdl(a1, lc) - c0;
      const srd_t psrd = __builtin_amdgcn_make_buffer_rsrc((void*)(((r & 1) ? V : K) + (size_t)c0 * ROWB), 0, (cl - 1) * ROWB + HEADB, 0x00020000);
      unsigned d0;
      asm volatile("s_nop 4\n\tbuffer_load_dword %0, %1, %2, %3 offen\n\tbuffer_load_dword %0, %1, %2, %3 offen offset:128\n\tbuffer_load_dword %0, %1, %2, %3 offen offset:256\n\ts_waitcnt vmcnt(0)"
                   : "=&v"(d0) : "v"(lane * ROWB), "s"(psrd), "s"((ts + HS) * (64 * ROWB)) : "memory");
    }
    while (__builtin_amdgcn_s_memrealtime() < tend) __builtin_amdgcn_s_sleep(2);
  }
  constexpr unsigned QSLOT = 0x45BC6780u;
  const unsigned half = (unsigned)w >> 2, kbeg = lds0 + rg * 3072;
  const unsigned role = DBL && nq == 5 ? (w >= 6 ? 1u : 0u) : half;
  if (!half) dma3(ksrd, kvo, 0u, kbeg + KOFS);
  for (int g = 4 * half; g < min(nq, 4 * (int)half + 4); ++g) dma3(qsrd, kvo, (unsigned)(qb0 + g) * (32 * ROWB), kbeg + ((QSLOT >> (4 * g)) & 15u) * BLKB);
  const int nb = nq <= 2 ? 0 : (nq <= 6 ? min(nq, 4) : 2 * nq - 8), p0 = nq <= 2 ? 0 : max(4, nb);
  const bool both = w < nb;
  const int qi = both ? w : (w >= p0 ? nb + ((w - p0) >> 1) : 8);
  const bool offl = OFFL && nb == nq && nq <= 4;
  const int kh = both ? 0 : (w & 1), partner = offl ? w ^ 4 : w ^ 1, qo = offl ? rg : qi;
  const bool act = qi < nq && kh < nkb;
  const int tailn = L - (nkb - 1) * 32;
  {
    const unsigned kk = kh && act ? BLKB : 0;
    const int nu = act ? (both ? nkb : (nkb + 1 - kh) >> 1) : 0;
    const bool ownlast = both || ((nkb - 1 - kh) & 1) == 0;
    auto region = [&](int x) { return lds0 + (unsigned)rfl(x < 4 ? VOFS + ((unsigned)(2 * S + 2 + x) % 6u) * BLKB : KOFS + ((unsigned)(2 * S + x - 4) % 6u) * BLKB); };
    const int nrow = qo < nq ? min(32, L - (qb0 + qo) * 32) : 0;
    const srd_t osrd = __builtin_amdgcn_make_buffer_rsrc(O + (size_t)(s0 - cu0 + (qb0 + qo) * 32) * ROWB, 0, nrow > 0 ? (nrow - 1) * ROWB + HEADB : 0, 0x00020000);
    u32x16 sa = {32u * ROWB, kbeg + KOFS + BLKB, 0u, kbeg + VOFS, 0u, kbeg + VOFS + 2 * BLKB, kbeg + STOFS, (unsigned)nkb, 1u, role,
                 ((QSLOT >> (4 * (qi & 7))) & 15u) * BLKB, KOFS + kk, (unsigned)(nu - 2), both ? 0u : 1u, 0u, ownlast ? (unsigned)tailn : 32u};
    u32x4 sb = {(unsigned)(act ? (both || nu == S ? 0 : 1) : S - 1), 2u * BLKB + kk, 2u * BLKB + kk, both ? 0u : (unsigned)BLKB};
    u32x4 sc = {role ? (nb == nq && nq <= 4 ? 0u : 1u) | (nq < 8 ? 2u : 0u) | (nq <= 5 ? 4u : 0u) | (DBL && nq == 5 ? 16u : 0u) : 0u, region(w), region(partner),
                both ? (offl ? 5u : 0u) : (qi < nq ? (kh ? 1u : 2u) : (offl && role && rg < nq ? 4u : 3u))};
    const unsigned stat = (unsigned)rfl(lds0 + STOFS + w * 512);
    const unsigned pstat = (unsigned)rfl(lds0 + STOFS + partner * 512);
#pragma unroll
    for (int i = 0; i < 16; ++i) sa[i] = (unsigned)rfl(sa[i]);
#pragma unroll
    for (int i = 0; i < 4; ++i) sc[i] = (unsigned)rfl(sc[i]);
#pragma unroll
    for (int i = 0; i < 4; ++i) sb[i] = (unsigned)rfl(sb[i]);
    va0 += 2 * BLKB + kk;
    u32x8 va = {ka0, ka1, ka2, ka3, va0, va0 ^ 64u, kvo, vvo};
    asm volatile(
      "\ts_mov_b32 s92, m0\n"
      "\ts_cmp_eq_u32 s81, 0\n"
      "\ts_cbranch_scc1 Lp0_%=\n"
      "\ts_bitcmp0_b32 s56, 4\n"
      "\ts_cbranch_scc1 Lp3_%=\n"
      "\tv_subrev_u32 v16, 98304, v8\n"
      "\tv_subrev_u32 v17, 98304, v9\n"
      "Lp3_%=:\n"
      "\ts_call_b64 s[60:61], Llk_%=\n"
      "\ts_call_b64 s[60:61], Llv_%=\n"
      "\ts_call_b64 s[60:61], Llk_%=\n"
      "\ts_call_b64 s[60:61], Llv_%=\n"
      "Lp0_%=:\n"
      "\ts_mov_b32 s93, 0x3dd53b95\n"
      "\tv_mov_b32 v10, 0x7f800000\n"
      "\tv_mov_b32 v11, 0\n"
      "\tv_mov_b32 v13, s98\n"
      "\tds_write_b32 v13, v11 offset:256\n"
      "\ts_mov_b32 s95, 0\n"
      "\tv_mbcnt_lo_u32_b32 v15, -1, 0\n"
      "\tv_mbcnt_hi_u32_b32 v15, -1, v15\n"
      "\tv_lshrrev_b32 v1, 3, v15\n"
      "\tv_and_b32 v1, 4, v1\n"
      "\tv_sub_u32 v1, s87, v1\n"
      "\tv_mov_b32 v12, 0x7fc00000\n"
      "\ts_cmp_eq_u32 s85, 0\n"
      "\ts_cselect_b32 s63, 24576, 0\n"
      "\ts_cselect_b32 s68, -49152, 0\n"
      "\ts_mov_b32 s62, 24576\n"
      "\ts_mov_b32 s96, 0\n"
      "\ts_or_b32 s69, s81, s85\n"
      "\tv_add_u32 v248, s82, v2\n"
      "\tv_add_u32 v249, s82, v3\n"
      "\tv_add_u32 v250, s82, v4\n"
      "\tv_add_u32 v251, s82, v5\n"
      "\tv_add_u32 v2, s83, v2\n"
      "\tv_add_u32 v3, s83, v3\n"
      "\tv_add_u32 v4, s83, v4\n"
      "\tv_add_u32 v5, s83, v5\n"
      "\ts_bitcmp1_b32 0x18, s59\n"
      "\ts_cbranch_scc1 Lp4_%=\n"
      "\tv_mov_b64 v[16:17], 0\n"
      "\tv_mov_b64 v[18:19], 0\n"
      "\tv_mov_b64 v[20:21], 0\n"
      "\tv_mov_b64 v[22:23], 0\n"
      "\tv_mov_b64 v[24:25], 0\n"
      "\tv_mov_b64 v[26:27], 0\n"
      "\tv_mov_b64 v[28:29], 0\n"
      "\tv_mov_b64 v[30:31], 0\n"
      "\tv_mov_b64 v[32:33], 0\n"
      "\tv_mov_b64 v[34:35], 0\n"
      "\tv_mov_b64 v[36:37], 0\n"
      "\tv_mov_b64 v[38:39], 0\n"
      "\tv_mov_b64 v[40:41], 0\n"
      "\tv_mov_b64 v[42:43], 0\n"
      "\tv_mov_b64 v[44:45], 0\n"
      "\tv_mov_b64 v[46:47], 0\n"
      "\tv_mov_b64 v[48:49], 0\n"
      "\tv_mov_b64 v[50:51], 0\n"
      "\tv_mov_b64 v[52:53], 0\n"
      "\tv_mov_b64 v[54:55], 0\n"
      "\tv_mov_b64 v[56:57], 0\n"
      "\tv_mov_b64 v[58:59], 0\n"
      "\tv_mov_b64 v[60:61], 0\n"
      "\tv_mov_b64 v[62:63], 0\n"
      "\tv_mov_b64 v[64:65], 0\n"
      "\tv_mov_b64 v[66:67], 0\n"
      "\tv_mov_b64 v[68:69], 0\n"
      "\tv_mov_b64 v[70:71], 0\n"
      "\tv_mov_b64 v[72:73], 0\n"
      "\tv_mov_b64 v[74:75], 0\n"
      "\tv_mov_b64 v[76:77], 0\n"
      "\tv_mov_b64 v[78:79], 0\n"
      "\tv_mov_b64 v[80:81], 0\n"
      "\tv_mov_b64 v[82:83], 0\n"
      "\tv_mov_b64 v[84:85], 0\n"
      "\tv_mov_b64 v[86:87], 0\n"
      "\tv_mov_b64 v[88:89], 0\n"
      "\tv_mov_b64 v[90:91], 0\n"
      "\tv_mov_b64 v[92:93], 0\n"
      "\tv_mov_b64 v[94:95], 0\n"
      "\tv_mov_b64 v[96:97], 0\n"
      "\tv_mov_b64 v[98:99], 0\n"
      "\tv_mov_b64 v[100:101], 0\n"
      "\tv_mov_b64 v[102:103], 0\n"
      "\tv_mov_b64 v[104:105], 0\n"
      "\tv_mov_b64 v[106:107], 0\n"
      "\tv_mov_b64 v[108:109], 0\n"
      "\tv_mov_b64 v[110:111], 0\n"
      "Lp4_%=:\n"
      "\ts_cmp_lg_u32 s81, 0\n"
      "\ts_cbranch_scc1 Lp5_%=\n"
      "\ts_setprio 2\n"
      "\ts_cmp_eq_u32 s85, 0\n"
      "\ts_waitcnt vmcnt(0) lgkmcnt(0)\n"
      "\ts_cbranch_scc1 Lp1_%=\n"
      "\ts_setprio 3\n"
      "\ts_branch Lp1_%=\n"
      "Lp5_%=:\n"
      "\ts_bitcmp0_b32 s56, 0\n"
      "\ts_waitcnt lgkmcnt(0)\n"
      "\ts_cbranch_scc1 Lp1_%=\n"
      "\ts_bitcmp1_b32 s56, 4\n"
      "\ts_waitcnt vmcnt(18)\n"
      "\ts_cbranch_scc1 2f\n"
      "\ts_waitcnt vmcnt(9)\n"
      "2:\n"
      "Lp1_%=:\n"
      "\ts_barrier\n"
      "\ts_bitcmp0_b32 s56, 1\n"
      "\ts_cbranch_scc1 Lp2_%=\n"
      "\ts_call_b64 s[60:61], Llk_%=\n"
      "Lp2_%=:\n"
      "\ts_cmp_lt_i32 s84, -1\n"
      "\ts_cbranch_scc1 Lf5_%=\n"
      "\tds_read_b128 v[112:115], v248 offset:0\n"
      "\tds_read_b128 v[192:195], v2 offset:0\n"
      "\tds_read_b128 v[116:119], v249 offset:0\n"
      "\tds_read_b128 v[196:199], v3 offset:0\n"
      "\tds_read_b128 v[120:123], v250 offset:0\n"
      "\tds_read_b128 v[200:203], v4 offset:0\n"
      "\tds_read_b128 v[124:127], v251 offset:0\n"
      "\tds_read_b128 v[204:207], v5 offset:0\n"
      "\tds_read_b128 v[128:131], v248 offset:1024\n"
      "\tds_read_b128 v[208:211], v2 offset:1024\n"
      "\tds_read_b128 v[132:135], v249 offset:1024\n"
      "\tds_read_b128 v[212:215], v3 offset:1024\n"
      "\tds_read_b128 v[136:139], v250 offset:1024\n"
      "\tds_read_b128 v[216:219], v4 offset:1024\n"
      "\ts_waitcnt lgkmcnt(12)\n"
      "\tv_mfma_f32_32x32x16_bf16 v[160:175], v[192:195], v[112:115], 0\n"
      "\tds_read_b128 v[140:143], v251 offset:1024\n"
      "\tds_read_b128 v[220:223], v5 offset:1024\n"
      "\ts_waitcnt lgkmcnt(12)\n"
      "\tv_mfma_f32_32x32x16_bf16 v[160:175], v[196:199], v[116:119], v[160:175]\n"
      "\tds_read_b128 v[144:147], v248 offset:2048\n"
      "\tds_read_b128 v[224:227], v2 offset:2048\n"
      "\ts_waitcnt lgkmcnt(12)\n"
      "\tv_mfma_f32_32x32x16_bf16 v[160:175], v[200:203], v[120:123], v[160:175]\n"
      "\tds_read_b128 v[148:151], v249 offset:2048\n"
      "\tds_read_b128 v[228:231], v3 offset:2048\n"
      "\ts_waitcnt lgkmcnt(12)\n"
      "\tv_mfma_f32_32x32x16_bf16 v[160:175], v[204:207], v[124:127], v[160:175]\n"
      "\tds_read_b128 v[152:155], v250 offset:2048\n"
      "\tds_read_b128 v[232:235], v4 offset:2048\n"
      "\ts_waitcnt lgkmcnt(12)\n"
      "\tv_mfma_f32_32x32x16_bf16 v[160:175], v[208:211], v[128:131], v[160:175]\n"
      "\tds_read_b128 v[156:159], v251 offset:2048\n"
      "\tds_read_b128 v[236:239], v5 offset:2048\n"
      "\ts_waitcnt lgkmcnt(12)\n"
      "\tv_mfma_f32_32x32x16_bf16 v[160:175], v[212:215], v[132:135], v[160:175]\n"
      "\ts_waitcnt lgkmcnt(10)\n"
      "\tv_mfma_f32_32x32x16_bf16 v[160:175], v[216:219], v[136:139], v[160:175]\n"
      "\ts_waitcnt lgkmcnt(8)\n"
      "\tv_mfma_f32_32x32x16_bf16 v[160:175], v[220:223], v[140:143], v[160:175]\n"
      "\ts_waitcnt lgkmcnt(6)\n"
      "\tv_mfma_f32_32x32x16_bf16 v[160:175], v[224:227], v[144:147], v[160:175]\n"
      "\ts_waitcnt lgkmcnt(4)\n"
      "\tv_mfma_f32_32x32x16_bf16 v[160:175], v[228:231], v[148:151], v[160:175]\n"
      "\ts_waitcnt lgkmcnt(2)\n"
      "\tv_mfma_f32_32x32x16_bf16 v[160:175], v[232:235], v[152:155], v[160:175]\n"
      "\ts_waitcnt lgkmcnt(0)\n"
      "\tv_mfma_f32_32x32x16_bf16 v[160:175], v[236:239], v[156:159], v[160:175]\n"
      "\tv_add_u32 v2, s91, v2\n"
      "\tv_add_u32 v3, s91, v3\n"
      "\tv_add_u32 v4, s91, v4\n"
      "\tv_add_u32 v5, s91, v5\n"
      "Lf5_%=:\n"
      "\ts_cmp_eq_u32 s85, 0\n"
      "\ts_cselect_b32 s97, s57, s58\n"
      "\tv_lshlrev_b32 v248, 6, v15\n"
      "\tv_bfe_u32 v249, v15, 1, 3\n"
      "\tv_lshl_add_u32 v248, v249, 3, v248\n"
      "\tv_add_u32 v248, s97, v248\n"
      "\tv_xor_b32 v249, 8, v248\n"
      "\tv_xor_b32 v250, 16, v248\n"
      "\tv_xor_b32 v251, 24, v248\n"
      "\tv_xor_b32 v252, 32, v248\n"
      "\tv_xor_b32 v253, 40, v248\n"
      "\tv_xor_b32 v254, 48, v248\n"
      "\tv_xor_b32 v255, 56, v248\n"
      "\ts_cmp_lt_i32 s84, 0\n"
      "\ts_cbranch_scc1 Lf0_%=\n"
      "\tv_max3_f32 v13, v160, v161, v162\n"
      "\tv_max3_f32 v14, v163, v164, v165\n"
      "\tv_max3_f32 v13, v13, v166, v167\n"
      "\tv_max3_f32 v14, v14, v168, v169\n"
      "\tv_max3_f32 v13, v13, v170, v171\n"
      "\tv_max3_f32 v14, v14, v172, v173\n"
      "\tv_max3_f32 v13, v13, v174, v175\n"
      "\tv_max_f32 v13, v13, v14\n"
      "\ts_call_b64 s[60:61], Lresc_%=\n"
      "\ts_mov_b32 s95, 0\n"
      "\ts_mov_b32 s84, s70\n"
      "\tv_fma_f32 v160, v160, s93, v10\n"
      "\tv_fma_f32 v161, v161, s93, v10\n"
      "\tv_exp_f32 v160, v160\n"
      "\tv_fma_f32 v162, v162, s93, v10\n"
      "\tv_exp_f32 v161, v161\n"
      "\tv_fma_f32 v163, v163, s93, v10\n"
      "\tv_exp_f32 v162, v162\n"
      "\tv_fma_f32 v164, v164, s93, v10\n"
      "\tv_exp_f32 v163, v163\n"
      "\tv_fma_f32 v165, v165, s93, v10\n"
      "\tv_exp_f32 v164, v164\n"
      "\tv_fma_f32 v166, v166, s93, v10\n"
      "\tv_exp_f32 v165, v165\n"
      "\tv_exp_f32 v166, v166\n"
      "\tv_fma_f32 v167, v167, s93, v10\n"
      "\tv_add_f32 v11, v11, v160\n"
      "\tv_add_f32 v11, v11, v161\n"
      "\tv_add_f32 v11, v11, v162\n"
      "\tv_add_f32 v11, v11, v163\n"
      "Lf0_%=:\n"
      "\ts_bitcmp0_b32 s56, 2\n"
      "\ts_cbranch_scc1 Lf6_%=\n"
      "\ts_call_b64 s[60:61], Llk_%=\n"
      "\ts_call_b64 s[60:61], Llv_%=\n"
      "\ts_bitcmp1_b32 s56, 4\n"
      "\ts_waitcnt vmcnt(12)\n"
      "\ts_cbranch_scc1 2f\n"
      "\ts_waitcnt vmcnt(6)\n"
      "2:\n"
      "\ts_branch Lf1_%=\n"
      "Lf6_%=:\n"
      "\ts_waitcnt vmcnt(0)\n"
      "Lf1_%=:\n"
      "\ts_cmp_eq_u32 s81, 0\n"
      "\ts_barrier\n"
      "\ts_cbranch_scc1 Lf3_%=\n"
      "Lf4_%=:\n"
      "\ts_add_u32 s97, s74, 1\n"
      "\ts_cmp_lt_u32 s97, s80\n"
      "\ts_cbranch_scc1 Lf9_%=\n"
      "\ts_call_b64 s[60:61], Llk_%=\n"
      "\ts_branch Lf4_%=\n"
      "Lf9_%=:\n"
      "\ts_call_b64 s[60:61], Llv_%=\n"
      "\ts_cmp_lt_u32 s74, 6\n"
      "\ts_cbranch_scc1 Lf4_%=\n"
      "Lf3_%=:\n"
      "\ts_cmp_lt_i32 s84, -1\n"
      "\ts_cbranch_scc1 Lidle_%=\n"
      "\ts_cmp_lt_i32 s84, 0\n"
      "\ts_cbranch_scc1 Lone_%=\n"
      "\tds_read_b128 v[192:195], v2 offset:12288\n"
      "\tds_read_b128 v[196:199], v3 offset:12288\n"
      "\tds_read_b128 v[200:203], v4 offset:12288\n"
      "\tds_read_b128 v[204:207], v5 offset:12288\n"
      "\tds_read_b128 v[208:211], v2 offset:13312\n"
      "\tds_read_b128 v[212:215], v3 offset:13312\n"
      "\tds_read_b128 v[216:219], v4 offset:13312\n"
      "\tds_read_b128 v[220:223], v5 offset:13312\n"
      "\tds_read_b128 v[224:227], v2 offset:14336\n"
      "\tds_read_b128 v[228:231], v3 offset:14336\n"
      "\tds_read_b128 v[232:235], v4 offset:14336\n"
      "\tds_read_b128 v[236:239], v5 offset:14336\n"
      "\ts_mov_b32 s96, s63\n"
      "\ts_waitcnt lgkmcnt(6)\n"
      "\t.p2align 6\n"
      "\ts_nop 0\n"
      "\ts_nop 0\n"
      "La0_%=:\n"
      "\tv_mfma_f32_32x32x16_bf16 v[176:191], v[192:195], v[112:115], 0\n"
      "\tds_read_b64_tr_b16 v[192:193], v6 offset:0\n"
      "\tds_read_b64_tr_b16 v[194:195], v6 offset:3072\n"
      "\tv_exp_f32 v167, v167\n"
      "\tv_fma_f32 v168, v168, s93, v10\n"
      "\tv_cvt_pk_bf16_f32 v240, v160, v161\n"
      "\tv_mfma_f32_32x32x16_bf16 v[176:191], v[196:199], v[116:119], v[176:191]\n"
      "\tds_read_b64_tr_b16 v[196:197], v7 offset:0\n"
      "\tds_read_b64_tr_b16 v[198:199], v7 offset:3072\n"
      "\tv_exp_f32 v168, v168\n"
      "\tv_fma_f32 v169, v169, s93, v10\n"
      "\tv_cvt_pk_bf16_f32 v241, v162, v163\n"
      "\tv_mfma_f32_32x32x16_bf16 v[176:191], v[200:203], v[120:123], v[176:191]\n"
      "\tds_read_b64_tr_b16 v[200:201], v6 offset:1024\n"
      "\tds_read_b64_tr_b16 v[202:203], v6 offset:4096\n"
      "\tv_exp_f32 v169, v169\n"
      "\tv_fma_f32 v170, v170, s93, v10\n"
      "\tv_cvt_pk_bf16_f32 v242, v164, v165\n"
      "\tv_mfma_f32_32x32x16_bf16 v[176:191], v[204:207], v[124:127], v[176:191]\n"
      "\tds_read_b64_tr_b16 v[204:205], v7 offset:1024\n"
      "\tds_read_b64_tr_b16 v[206:207], v7 offset:4096\n"
      "\tv_exp_f32 v170, v170\n"
      "\tv_fma_f32 v171, v171, s93, v10\n"
      "\tv_cvt_pk_bf16_f32 v243, v166, v167\n"
      "\tv_mfma_f32_32x32x16_bf16 v[176:191], v[208:211], v[128:131], v[176:191]\n"
      "\tds_read_b64_tr_b16 v[208:209], v6 offset:2048\n"
      "\tds_read_b64_tr_b16 v[210:211], v6 offset:5120\n"
      "\tv_exp_f32 v171, v171\n"
      "\tv_fma_f32 v172, v172, s93, v10\n"
      "\tv_cvt_pk_bf16_f32 v244, v168, v169\n"
      "\tv_mfma_f32_32x32x16_bf16 v[176:191], v[212:215], v[132:135], v[176:191]\n"
      "\tds_read_b64_tr_b16 v[212:213], v7 offset:2048\n"
      "\tds_read_b64_tr_b16 v[214:215], v7 offset:5120\n"
      "\tv_exp_f32 v172, v172\n"
      "\tv_fma_f32 v173, v173, s93, v10\n"
      "\ts_waitcnt lgkmcnt(12)\n"
      "\tv_mfma_f32_32x32x16_bf16 v[176:191], v[216:219], v[136:139], v[176:191]\n"
      "\tds_read_b64_tr_b16 v[216:217], v6 offset:6144\n"
      "\tds_read_b64_tr_b16 v[218:219], v6 offset:9216\n"
      "\tv_exp_f32 v173, v173\n"
      "\tv_fma_f32 v174, v174, s93, v10\n"
      "\tv_cvt_pk_bf16_f32 v245, v170, v171\n"
      "\tv_mfma_f32_32x32x16_bf16 v[176:191], v[220:223], v[140:143], v[176:191]\n"
      "\tds_read_b64_tr_b16 v[220:221], v7 offset:6144\n"
      "\tds_read_b64_tr_b16 v[222:223], v7 offset:9216\n"
      "\tv_exp_f32 v174, v174\n"
      "\tv_fma_f32 v175, v175, s93, v10\n"
      "\tv_add_f32 v11, v11, v164\n"
      "\tv_mfma_f32_32x32x16_bf16 v[176:191], v[224:227], v[144:147], v[176:191]\n"
      "\tds_read_b64_tr_b16 v[224:225], v6 offset:7168\n"
      "\tds_read_b64_tr_b16 v[226:227], v6 offset:10240\n"
      "\tv_exp_f32 v175, v175\n"
      "\tv_cvt_pk_bf16_f32 v246, v172, v173\n"
      "\tv_add_f32 v11, v11, v165\n"
      "\tv_mfma_f32_32x32x16_bf16 v[176:191], v[228:231], v[148:151], v[176:191]\n"
      "\tds_read_b64_tr_b16 v[228:229], v7 offset:7168\n"
      "\tds_read_b64_tr_b16 v[230:231], v7 offset:10240\n"
      "\tv_add_f32 v11, v11, v166\n"
      "\tv_add_f32 v11, v11, v167\n"
      "\tv_mfma_f32_32x32x16_bf16 v[176:191], v[232:235], v[152:155], v[176:191]\n"
      "\tds_read_b64_tr_b16 v[232:233], v6 offset:8192\n"
      "\tds_read_b64_tr_b16 v[234:235], v6 offset:11264\n"
      "\tv_cvt_pk_bf16_f32 v247, v174, v175\n"
      "\tv_add_f32 v11, v11, v168\n"
      "\ts_sub_u32 s84, s84, 1\n"
      "\tv_mfma_f32_32x32x16_bf16 v[176:191], v[236:239], v[156:159], v[176:191]\n"
      "\tds_read_b64_tr_b16 v[236:237], v7 offset:8192\n"
      "\tds_read_b64_tr_b16 v[238:239], v7 offset:11264\n"
      "\tv_add_f32 v11, v11, v169\n"
      "\ts_waitcnt lgkmcnt(14)\n"
      "\ts_cbranch_scc1 Lx0_%=\n"
      "Lb0_%=:\n"
      "\tv_mfma_f32_32x32x16_bf16 v[16:31], v[192:195], v[240:243], v[16:31]\n"
      "\tv_add_f32 v11, v11, v170\n"
      "\tv_add_u32 v2, s96, v2\n"
      "\tv_add_f32 v11, v11, v171\n"
      "\tv_add_u32 v3, s96, v3\n"
      "\tv_add_f32 v11, v11, v172\n"
      "\tv_mfma_f32_32x32x16_bf16 v[32:47], v[196:199], v[240:243], v[32:47]\n"
      "\tv_add_u32 v4, s96, v4\n"
      "\tv_add_f32 v11, v11, v173\n"
      "\tv_max3_f32 v13, v176, v177, v178\n"
      "\tv_max3_f32 v14, v179, v180, v181\n"
      "\tv_add_f32 v11, v11, v174\n"
      "\tv_mfma_f32_32x32x16_bf16 v[48:63], v[200:203], v[240:243], v[48:63]\n"
      "\tv_max3_f32 v13, v13, v182, v183\n"
      "\tv_max3_f32 v14, v14, v184, v185\n"
      "\tv_add_u32 v5, s96, v5\n"
      "\tv_max3_f32 v13, v13, v186, v187\n"
      "\tv_max3_f32 v14, v14, v188, v189\n"
      "\tv_mfma_f32_32x32x16_bf16 v[64:79], v[204:207], v[240:243], v[64:79]\n"
      "\tv_max3_f32 v13, v13, v190, v191\n"
      "\tv_add_f32 v11, v11, v175\n"
      "\tv_max_f32 v13, v13, v14\n"
      "\ts_cmp_lg_u32 s85, 0\n"
      "\tv_cmp_nle_f32 vcc, v13, v12\n"
      "\ts_waitcnt lgkmcnt(0)\n"
      "\ts_cbranch_scc1 Lsy0_%=\n"
      "Lsr0_%=:\n"
      "\tv_mfma_f32_32x32x16_bf16 v[80:95], v[208:211], v[240:243], v[80:95]\n"
      "\tds_read_b128 v[192:195], v2 offset:0\n"
      "\tds_read_b128 v[196:199], v3 offset:0\n"
      "\ts_cbranch_vccnz Lrs0_%=\n"
      "Lrr0_%=:\n"
      "\tv_fma_f32 v176, v176, s93, v10\n"
      "\tv_mfma_f32_32x32x16_bf16 v[96:111], v[212:215], v[240:243], v[96:111]\n"
      "\tds_read_b128 v[200:203], v4 offset:0\n"
      "\tds_read_b128 v[204:207], v5 offset:0\n"
      "\tv_exp_f32 v176, v176\n"
      "\tv_fma_f32 v177, v177, s93, v10\n"
      "\tv_mfma_f32_32x32x16_bf16 v[16:31], v[216:219], v[244:247], v[16:31]\n"
      "\tds_read_b128 v[208:211], v2 offset:1024\n"
      "\tds_read_b128 v[212:215], v3 offset:1024\n"
      "\tv_exp_f32 v177, v177\n"
      "\tv_fma_f32 v178, v178, s93, v10\n"
      "\tv_mfma_f32_32x32x16_bf16 v[32:47], v[220:223], v[244:247], v[32:47]\n"
      "\tds_read_b128 v[216:219], v4 offset:1024\n"
      "\tds_read_b128 v[220:223], v5 offset:1024\n"
      "\tv_exp_f32 v178, v178\n"
      "\tv_fma_f32 v179, v179, s93, v10\n"
      "\tv_add_f32 v11, v11, v176\n"
      "\tv_mfma_f32_32x32x16_bf16 v[48:63], v[224:227], v[244:247], v[48:63]\n"
      "\tds_read_b128 v[224:227], v2 offset:2048\n"
      "\tv_exp_f32 v179, v179\n"
      "\tv_fma_f32 v180, v180, s93, v10\n"
      "\tv_add_f32 v11, v11, v177\n"
      "\tv_mfma_f32_32x32x16_bf16 v[64:79], v[228:231], v[244:247], v[64:79]\n"
      "\tds_read_b128 v[228:231], v3 offset:2048\n"
      "\tv_exp_f32 v180, v180\n"
      "\tv_fma_f32 v181, v181, s93, v10\n"
      "\tv_add_f32 v11, v11, v178\n"
      "\tv_mfma_f32_32x32x16_bf16 v[80:95], v[232:235], v[244:247], v[80:95]\n"
      "\tds_read_b128 v[232:235], v4 offset:2048\n"
      "\tv_exp_f32 v181, v181\n"
      "\tv_fma_f32 v182, v182, s93, v10\n"
      "\tv_add_f32 v11, v11, v179\n"
      "\tv_mfma_f32_32x32x16_bf16 v[96:111], v[236:239], v[244:247], v[96:111]\n"
      "\tds_read_b128 v[236:239], v5 offset:2048\n"
      "\tv_exp_f32 v182, v182\n"
      "\tv_fma_f32 v183, v183, s93, v10\n"
      "\ts_waitcnt lgkmcnt(6)\n"
      "La1_%=:\n"
      "\tv_mfma_f32_32x32x16_bf16 v[160:175], v[192:195], v[112:115], 0\n"
      "\tds_read_b64_tr_b16 v[192:193], v6 offset:12288\n"
      "\tds_read_b64_tr_b16 v[194:195], v6 offset:15360\n"
      "\tv_exp_f32 v183, v183\n"
      "\tv_fma_f32 v184, v184, s93, v10\n"
      "\tv_cvt_pk_bf16_f32 v240, v176, v177\n"
      "\tv_mfma_f32_32x32x16_bf16 v[160:175], v[196:199], v[116:119], v[160:175]\n"
      "\tds_read_b64_tr_b16 v[196:197], v7 offset:12288\n"
      "\tds_read_b64_tr_b16 v[198:199], v7 offset:15360\n"
      "\tv_exp_f32 v184, v184\n"
      "\tv_fma_f32 v185, v185, s93, v10\n"
      "\tv_cvt_pk_bf16_f32 v241, v178, v179\n"
      "\tv_mfma_f32_32x32x16_bf16 v[160:175], v[200:203], v[120:123], v[160:175]\n"
      "\tds_read_b64_tr_b16 v[200:201], v6 offset:13312\n"
      "\tds_read_b64_tr_b16 v[202:203], v6 offset:16384\n"
      "\tv_exp_f32 v185, v185\n"
      "\tv_fma_f32 v186, v186, s93, v10\n"
      "\tv_cvt_pk_bf16_f32 v242, v180, v181\n"
      "\tv_mfma_f32_32x32x16_bf16 v[160:175], v[204:207], v[124:127], v[160:175]\n"
      "\tds_read_b64_tr_b16 v[204:205], v7 offset:13312\n"
      "\tds_read_b64_tr_b16 v[206:207], v7 offset:16384\n"
      "\tv_exp_f32 v186, v186\n"
      "\tv_fma_f32 v187, v187, s93, v10\n"
      "\tv_cvt_pk_bf16_f32 v243, v182, v183\n"
      "\tv_mfma_f32_32x32x16_bf16 v[160:175], v[208:211], v[128:131], v[160:175]\n"
      "\tds_read_b64_tr_b16 v[208:209], v6 offset:14336\n"
      "\tds_read_b64_tr_b16 v[210:211], v6 offset:17408\n"
      "\tv_exp_f32 v187, v187\n"
      "\tv_fma_f32 v188, v188, s93, v10\n"
      "\tv_cvt_pk_bf16_f32 v244, v184, v185\n"
      "\tv_mfma_f32_32x32x16_bf16 v[160:175], v[212:215], v[132:135], v[160:175]\n"
      "\tds_read_b64_tr_b16 v[212:213], v7 offset:14336\n"
      "\tds_read_b64_tr_b16 v[214:215], v7 offset:17408\n"
      "\tv_exp_f32 v188, v188\n"
      "\tv_fma_f32 v189, v189, s93, v10\n"
      "\ts_waitcnt lgkmcnt(12)\n"
      "\tv_mfma_f32_32x32x16_bf16 v[160:175], v[216:219], v[136:139], v[160:175]\n"
      "\tds_read_b64_tr_b16 v[216:217], v6 offset:18432\n"
      "\tds_read_b64_tr_b16 v[218:219], v6 offset:21504\n"
      "\tv_exp_f32 v189, v189\n"
      "\tv_fma_f32 v190, v190, s93, v10\n"
      "\tv_cvt_pk_bf16_f32 v245, v186, v187\n"
      "\tv_mfma_f32_32x32x16_bf16 v[160:175], v[220:223], v[140:143], v[160:175]\n"
      "\tds_read_b64_tr_b16 v[220:221], v7 offset:18432\n"
      "\tds_read_b64_tr_b16 v[222:223], v7 offset:21504\n"
      "\tv_exp_f32 v190, v190\n"
      "\tv_fma_f32 v191, v191, s93, v10\n"
      "\tv_add_f32 v11, v11, v180\n"
      "\tv_mfma_f32_32x32x16_bf16 v[160:175], v[224:227], v[144:147], v[160:175]\n"
      "\tds_read_b64_tr_b16 v[224:225], v6 offset:19456\n"
      "\tds_read_b64_tr_b16 v[226:227], v6 offset:22528\n"
      "\tv_exp_f32 v191, v191\n"
      "\tv_cvt_pk_bf16_f32 v246, v188, v189\n"
      "\tv_add_f32 v11, v11, v181\n"
      "\tv_mfma_f32_32x32x16_bf16 v[160:175], v[228:231], v[148:151], v[160:175]\n"
      "\tds_read_b64_tr_b16 v[228:229], v7 offset:19456\n"
      "\tds_read_b64_tr_b16 v[230:231], v7 offset:22528\n"
      "\tv_add_f32 v11, v11, v182\n"
      "\tv_add_f32 v11, v11, v183\n"
      "\tv_mfma_f32_32x32x16_bf16 v[160:175], v[232:235], v[152:155], v[160:175]\n"
      "\tds_read_b64_tr_b16 v[232:233], v6 offset:20480\n"
      "\tds_read_b64_tr_b16 v[234:235], v6 offset:23552\n"
      "\tv_cvt_pk_bf16_f32 v247, v190, v191\n"
      "\tv_add_f32 v11, v11, v184\n"
      "\ts_sub_u32 s84, s84, 1\n"
      "\tv_mfma_f32_32x32x16_bf16 v[160:175], v[236:239], v[156:159], v[160:175]\n"
      "\tds_read_b64_tr_b16 v[236:237], v7 offset:20480\n"
      "\tds_read_b64_tr_b16 v[238:239], v7 offset:23552\n"
      "\tv_add_f32 v11, v11, v185\n"
      "\ts_waitcnt lgkmcnt(14)\n"
      "\ts_cbranch_scc1 Lx1_%=\n"
      "Lb1_%=:\n"
      "\tv_mfma_f32_32x32x16_bf16 v[16:31], v[192:195], v[240:243], v[16:31]\n"
      "\tv_add_f32 v11, v11, v186\n"
      "\ts_cmp_eq_u32 s62, 49152\n"
      "\tv_add_f32 v11, v11, v187\n"
      "\ts_cselect_b32 s96, s68, s63\n"
      "\tv_add_f32 v11, v11, v188\n"
      "\tv_mfma_f32_32x32x16_bf16 v[32:47], v[196:199], v[240:243], v[32:47]\n"
      "\ts_add_u32 s62, s62, s96\n"
      "\tv_add_f32 v11, v11, v189\n"
      "\tv_max3_f32 v13, v160, v161, v162\n"
      "\tv_max3_f32 v14, v163, v164, v165\n"
      "\tv_add_f32 v11, v11, v190\n"
      "\tv_mfma_f32_32x32x16_bf16 v[48:63], v[200:203], v[240:243], v[48:63]\n"
      "\tv_max3_f32 v13, v13, v166, v167\n"
      "\tv_max3_f32 v14, v14, v168, v169\n"
      "\tv_add_u32 v6, s96, v6\n"
      "\tv_max3_f32 v13, v13, v170, v171\n"
      "\tv_max3_f32 v14, v14, v172, v173\n"
      "\tv_mfma_f32_32x32x16_bf16 v[64:79], v[204:207], v[240:243], v[64:79]\n"
      "\tv_add_u32 v7, s96, v7\n"
      "\tv_max3_f32 v13, v13, v174, v175\n"
      "\tv_add_f32 v11, v11, v191\n"
      "\tv_max_f32 v13, v13, v14\n"
      "\ts_cmp_lg_u32 s69, 0\n"
      "\tv_cmp_nle_f32 vcc, v13, v12\n"
      "\ts_waitcnt lgkmcnt(0)\n"
      "\ts_cbranch_scc1 Lsy1_%=\n"
      "\ts_barrier\n"
      "Lsr1_%=:\n"
      "\tv_mfma_f32_32x32x16_bf16 v[80:95], v[208:211], v[240:243], v[80:95]\n"
      "\tds_read_b128 v[192:195], v2 offset:12288\n"
      "\tds_read_b128 v[196:199], v3 offset:12288\n"
      "\ts_cbranch_vccnz Lrs1_%=\n"
      "Lrr1_%=:\n"
      "\tv_fma_f32 v160, v160, s93, v10\n"
      "\tv_mfma_f32_32x32x16_bf16 v[96:111], v[212:215], v[240:243], v[96:111]\n"
      "\tds_read_b128 v[200:203], v4 offset:12288\n"
      "\tds_read_b128 v[204:207], v5 offset:12288\n"
      "\tv_exp_f32 v160, v160\n"
      "\tv_fma_f32 v161, v161, s93, v10\n"
      "\tv_mfma_f32_32x32x16_bf16 v[16:31], v[216:219], v[244:247], v[16:31]\n"
      "\tds_read_b128 v[208:211], v2 offset:13312\n"
      "\tds_read_b128 v[212:215], v3 offset:13312\n"
      "\tv_exp_f32 v161, v161\n"
      "\tv_fma_f32 v162, v162, s93, v10\n"
      "\tv_mfma_f32_32x32x16_bf16 v[32:47], v[220:223], v[244:247], v[32:47]\n"
      "\tds_read_b128 v[216:219], v4 offset:13312\n"
      "\tds_read_b128 v[220:223], v5 offset:13312\n"
      "\tv_exp_f32 v162, v162\n"
      "\tv_fma_f32 v163, v163, s93, v10\n"
      "\tv_add_f32 v11, v11, v160\n"
      "\tv_mfma_f32_32x32x16_bf16 v[48:63], v[224:227], v[244:247], v[48:63]\n"
      "\tds_read_b128 v[224:227], v2 offset:14336\n"
      "\tv_exp_f32 v163, v163\n"
      "\tv_fma_f32 v164, v164, s93, v10\n"
      "\tv_add_f32 v11, v11, v161\n"
      "\tv_mfma_f32_32x32x16_bf16 v[64:79], v[228:231], v[244:247], v[64:79]\n"
      "\tds_read_b128 v[228:231], v3 offset:14336\n"
      "\tv_exp_f32 v164, v164\n"
      "\tv_fma_f32 v165, v165, s93, v10\n"
      "\tv_add_f32 v11, v11, v162\n"
      "\tv_mfma_f32_32x32x16_bf16 v[80:95], v[232:235], v[244:247], v[80:95]\n"
      "\tds_read_b128 v[232:235], v4 offset:14336\n"
      "\tv_exp_f32 v165, v165\n"
      "\tv_fma_f32 v166, v166, s93, v10\n"
      "\tv_add_f32 v11, v11, v163\n"
      "\tv_mfma_f32_32x32x16_bf16 v[96:111], v[236:239], v[244:247], v[96:111]\n"
      "\tds_read_b128 v[236:239], v5 offset:14336\n"
      "\tv_exp_f32 v166, v166\n"
      "\tv_fma_f32 v167, v167, s93, v10\n"
      "\ts_waitcnt lgkmcnt(6)\n"
      "\ts_branch La0_%=\n"
      "Lx0_%=:\n"
      "\ts_cmp_eq_u32 s95, 0\n"
      "\ts_cbranch_scc1 Lbf0_%=\n"
      "\ts_call_b64 s[60:61], Lscale_%=\n"
      "\ts_sub_u32 s84, s70, 1\n"
      "\ts_cbranch_scc0 Lb0_%=\n"
      "\ts_branch Lbf0_%=\n"
      "Lsy0_%=:\n"
      "\ts_bitcmp0_b32 s56, 0\n"
      "\ts_waitcnt vmcnt(12)\n"
      "\ts_cbranch_scc1 1f\n"
      "\ts_waitcnt vmcnt(9)\n"
      "1:\n"
      "\ts_barrier\n"
      "\ts_cmp_eq_u32 s85, 0\n"
      "\ts_cbranch_scc1 Lsw0_%=\n"
      "\ts_cmp_ge_u32 s90, 49152\n"
      "\ts_mov_b32 s97, 24576\n"
      "\ts_cselect_b32 s97, -49152, s97\n"
      "\ts_add_u32 s90, s90, s97\n"
      "\ts_add_u32 s97, s97, -12288\n"
      "\tv_add_u32 v6, s97, v6\n"
      "\tv_add_u32 v7, s97, v7\n"
      "\ts_cmp_ge_u32 s89, 49152\n"
      "\ts_mov_b32 s97, 24576\n"
      "\ts_cselect_b32 s97, -49152, s97\n"
      "\ts_add_u32 s89, s89, s97\n"
      "\ts_add_u32 s97, s97, 12288\n"
      "\tv_add_u32 v2, s97, v2\n"
      "\tv_add_u32 v3, s97, v3\n"
      "\tv_add_u32 v4, s97, v4\n"
      "\tv_add_u32 v5, s97, v5\n"
      "\ts_cmp_eq_u32 s81, 0\n"
      "\ts_cbranch_scc1 Lsr0_%=\n"
      "Lsw0_%=:\n"
      "\ts_call_b64 s[60:61], Llk_%=\n"
      "\ts_call_b64 s[60:61], Llv_%=\n"
      "\ts_call_b64 s[60:61], Llk_%=\n"
      "\ts_call_b64 s[60:61], Llv_%=\n"
      "\ts_branch Lsr0_%=\n"
      "Lrs0_%=:\n"
      "\ts_call_b64 s[60:61], Lresc_%=\n"
      "\ts_branch Lrr0_%=\n"
      "Lsy0f_%=:\n"
      "\ts_bitcmp0_b32 s56, 0\n"
      "\ts_waitcnt vmcnt(12)\n"
      "\ts_cbranch_scc1 1f\n"
      "\ts_waitcnt vmcnt(9)\n"
      "1:\n"
      "\ts_barrier\n"
      "\ts_cmp_eq_u32 s85, 0\n"
      "\ts_cbranch_scc1 Lsw0f_%=\n"
      "\ts_cmp_ge_u32 s90, 49152\n"
      "\ts_mov_b32 s97, 24576\n"
      "\ts_cselect_b32 s97, -49152, s97\n"
      "\ts_add_u32 s90, s90, s97\n"
      "\ts_add_u32 s97, s97, -12288\n"
      "\tv_add_u32 v6, s97, v6\n"
      "\tv_add_u32 v7, s97, v7\n"
      "\ts_cmp_ge_u32 s89, 49152\n"
      "\ts_mov_b32 s97, 24576\n"
      "\ts_cselect_b32 s97, -49152, s97\n"
      "\ts_add_u32 s89, s89, s97\n"
      "\ts_add_u32 s97, s97, 12288\n"
      "\tv_add_u32 v2, s97, v2\n"
      "\tv_add_u32 v3, s97, v3\n"
      "\tv_add_u32 v4, s97, v4\n"
      "\tv_add_u32 v5, s97, v5\n"
      "\ts_cmp_eq_u32 s81, 0\n"
      "\ts_cbranch_scc1 Lsr0f_%=\n"
      "Lsw0f_%=:\n"
      "\ts_call_b64 s[60:61], Llk_%=\n"
      "\ts_call_b64 s[60:61], Llv_%=\n"
      "\ts_call_b64 s[60:61], Llk_%=\n"
      "\ts_call_b64 s[60:61], Llv_%=\n"
      "\ts_branch Lsr0f_%=\n"
      "Lrs0f_%=:\n"
      "\ts_call_b64 s[60:61], Lresc_%=\n"
      "\ts_branch Lrr0f_%=\n"
      "Lx1_%=:\n"
      "\ts_cmp_eq_u32 s95, 0\n"
      "\ts_cbranch_scc1 Lbf1_%=\n"
      "\ts_call_b64 s[60:61], Lscale_%=\n"
      "\ts_sub_u32 s84, s70, 1\n"
      "\ts_cbranch_scc0 Lb1_%=\n"
      "\ts_branch Lbf1_%=\n"
      "Lsy1_%=:\n"
      "\ts_bitcmp0_b32 s56, 0\n"
      "\ts_waitcnt vmcnt(12)\n"
      "\ts_cbranch_scc1 1f\n"
      "\ts_waitcnt vmcnt(9)\n"
      "1:\n"
      "\ts_barrier\n"
      "\ts_cmp_eq_u32 s85, 0\n"
      "\ts_cbranch_scc1 Lsw1_%=\n"
      "\ts_cmp_ge_u32 s90, 49152\n"
      "\ts_mov_b32 s97, 24576\n"
      "\ts_cselect_b32 s97, -49152, s97\n"
      "\ts_add_u32 s90, s90, s97\n"
      "\ts_add_u32 s97, s97, 12288\n"
      "\tv_add_u32 v6, s97, v6\n"
      "\tv_add_u32 v7, s97, v7\n"
      "\ts_cmp_ge_u32 s89, 49152\n"
      "\ts_mov_b32 s97, 24576\n"
      "\ts_cselect_b32 s97, -49152, s97\n"
      "\ts_add_u32 s89, s89, s97\n"
      "\ts_add_u32 s97, s97, -12288\n"
      "\tv_add_u32 v2, s97, v2\n"
      "\tv_add_u32 v3, s97, v3\n"
      "\tv_add_u32 v4, s97, v4\n"
      "\tv_add_u32 v5, s97, v5\n"
      "\ts_cmp_eq_u32 s81, 0\n"
      "\ts_cbranch_scc1 Lsr1_%=\n"
      "Lsw1_%=:\n"
      "\ts_call_b64 s[60:61], Llk_%=\n"
      "\ts_call_b64 s[60:61], Llv_%=\n"
      "\ts_call_b64 s[60:61], Llk_%=\n"
      "\ts_call_b64 s[60:61], Llv_%=\n"
      "\ts_branch Lsr1_%=\n"
      "Lrs1_%=:\n"
      "\ts_call_b64 s[60:61], Lresc_%=\n"
      "\ts_branch Lrr1_%=\n"
      "Lsy1f_%=:\n"
      "\ts_bitcmp0_b32 s56, 0\n"
      "\ts_waitcnt vmcnt(12)\n"
      "\ts_cbranch_scc1 1f\n"
      "\ts_waitcnt vmcnt(9)\n"
      "1:\n"
      "\ts_barrier\n"
      "\ts_cmp_eq_u32 s85, 0\n"
      "\ts_cbranch_scc1 Lsw1f_%=\n"
      "\ts_cmp_ge_u32 s90, 49152\n"
      "\ts_mov_b32 s97, 24576\n"
      "\ts_cselect_b32 s97, -49152, s97\n"
      "\ts_add_u32 s90, s90, s97\n"
      "\ts_add_u32 s97, s97, 12288\n"
      "\tv_add_u32 v6, s97, v6\n"
      "\tv_add_u32 v7, s97, v7\n"
      "\ts_cmp_ge_u32 s89, 49152\n"
      "\ts_mov_b32 s97, 24576\n"
      "\ts_cselect_b32 s97, -49152, s97\n"
      "\ts_add_u32 s89, s89, s97\n"
      "\ts_add_u32 s97, s97, -12288\n"
      "\tv_add_u32 v2, s97, v2\n"
      "\tv_add_u32 v3, s97, v3\n"
      "\tv_add_u32 v4, s97, v4\n"
      "\tv_add_u32 v5, s97, v5\n"
      "\ts_cmp_eq_u32 s81, 0\n"
      "\ts_cbranch_scc1 Lsr1f_%=\n"
      "Lsw1f_%=:\n"
      "\ts_call_b64 s[60:61], Llk_%=\n"
      "\ts_call_b64 s[60:61], Llv_%=\n"
      "\ts_call_b64 s[60:61], Llk_%=\n"
      "\ts_call_b64 s[60:61], Llv_%=\n"
      "\ts_branch Lsr1f_%=\n"
      "Lrs1f_%=:\n"
      "\ts_call_b64 s[60:61], Lresc_%=\n"
      "\ts_branch Lrr1f_%=\n"
      "Llk_%=:\n"
      "\ts_cmp_lt_i32 s80, s79\n"
      "\ts_cselect_b32 m0, s73, 258048\n"
      "\ts_cselect_b32 s71, s72, 0x7f000000\n"
      "\tbuffer_load_dwordx4 v8, %[ksrd], s71 offen lds\n"
      "\ts_add_u32 m0, m0, 896\n"
      "\ts_add_u32 s73, s73, 12288\n"
      "\ts_sub_u32 s97, s73, 73728\n"
      "\ts_cmp_ge_u32 s73, s75\n"
      "\ts_cselect_b32 s73, s97, s73\n"
      "\tbuffer_load_dwordx4 v8, %[ksrd], s71 offen offset:128 lds\n"
      "\ts_add_u32 m0, m0, 896\n"
      "\ts_add_u32 s72, s72, 196608\n"
      "\tbuffer_load_dwordx4 v8, %[ksrd], s71 offen offset:256 lds\n"
      "\ts_add_u32 s80, s80, 1\n"
      "\ts_bitcmp1_b32 s56, 4\n"
      "\ts_cbranch_scc1 1f\n"
      "\ts_setpc_b64 s[60:61]\n"
      "1:\n"
      "\ts_sub_u32 m0, m0, 7936\n"
      "\ts_nop 0\n"
      "\tbuffer_load_dwordx4 v16, %[ksrd], s71 offen lds\n"
      "\ts_add_u32 m0, m0, 896\n"
      "\ts_nop 0\n"
      "\tbuffer_load_dwordx4 v16, %[ksrd], s71 offen offset:128 lds\n"
      "\ts_add_u32 m0, m0, 896\n"
      "\ts_nop 0\n"
      "\tbuffer_load_dwordx4 v16, %[ksrd], s71 offen offset:256 lds\n"
      "\ts_setpc_b64 s[60:61]\n"
      "Llv_%=:\n"
      "\ts_cmp_lt_i32 s74, s79\n"
      "\ts_cselect_b32 m0, s77, 258048\n"
      "\ts_cselect_b32 s71, s76, 0x7f000000\n"
      "\tbuffer_load_dwordx4 v9, %[vsrd], s71 offen lds\n"
      "\ts_add_u32 m0, m0, 896\n"
      "\ts_add_u32 s77, s77, 12288\n"
      "\ts_sub_u32 s97, s77, 73728\n"
      "\ts_cmp_ge_u32 s77, s78\n"
      "\ts_cselect_b32 s77, s97, s77\n"
      "\tbuffer_load_dwordx4 v9, %[vsrd], s71 offen offset:128 lds\n"
      "\ts_add_u32 m0, m0, 896\n"
      "\ts_add_u32 s76, s76, 196608\n"
      "\tbuffer_load_dwordx4 v9, %[vsrd], s71 offen offset:256 lds\n"
      "\ts_add_u32 s74, s74, 1\n"
      "\ts_bitcmp1_b32 s56, 4\n"
      "\ts_cbranch_scc1 1f\n"
      "\ts_setpc_b64 s[60:61]\n"
      "1:\n"
      "\ts_sub_u32 m0, m0, 7936\n"
      "\ts_nop 0\n"
      "\tbuffer_load_dwordx4 v17, %[vsrd], s71 offen lds\n"
      "\ts_add_u32 m0, m0, 896\n"
      "\ts_nop 0\n"
      "\tbuffer_load_dwordx4 v17, %[vsrd], s71 offen offset:128 lds\n"
      "\ts_add_u32 m0, m0, 896\n"
      "\ts_nop 0\n"
      "\tbuffer_load_dwordx4 v17, %[vsrd], s71 offen offset:256 lds\n"
      "\ts_setpc_b64 s[60:61]\n"
      "Lresc_%=:\n"
      "\tv_mov_b32 v14, v13\n"
      "\ts_nop 1\n"
      "\tv_permlane32_swap_b32 v13, v14\n"
      "\ts_nop 1\n"
      "\tv_max_f32 v13, v13, v14\n"
      "\tv_mul_f32 v13, s93, v13\n"
      "\tv_max_f32_e64 v13, v13, -v10\n"
      "\tv_add_f32 v15, v10, v13\n"
      "\tv_exp_f32_e64 v15, -v15\n"
      "\tv_sub_f32 v10, 0, v13\n"
      "\tv_add_f32 v12, 0x41000000, v13\n"
      "\tv_mul_f32 v12, 0x4119ac27, v12\n"
      "\tv_mul_f32 v11, v15, v11\n"
      "\ts_mov_b32 s95, 1\n"
      "\ts_mov_b32 s70, s84\n"
      "\ts_mov_b32 s84, 0\n"
      "\ts_setpc_b64 s[60:61]\n"
      "Lscale_%=:\n"
      "\ts_mov_b32 s95, 0\n"
      "\tv_mul_f32 v16, v15, v16\n"
      "\tv_mul_f32 v17, v15, v17\n"
      "\tv_mul_f32 v18, v15, v18\n"
      "\tv_mul_f32 v19, v15, v19\n"
      "\tv_mul_f32 v20, v15, v20\n"
      "\tv_mul_f32 v21, v15, v21\n"
      "\tv_mul_f32 v22, v15, v22\n"
      "\tv_mul_f32 v23, v15, v23\n"
      "\tv_mul_f32 v24, v15, v24\n"
      "\tv_mul_f32 v25, v15, v25\n"
      "\tv_mul_f32 v26, v15, v26\n"
      "\tv_mul_f32 v27, v15, v27\n"
      "\tv_mul_f32 v28, v15, v28\n"
      "\tv_mul_f32 v29, v15, v29\n"
      "\tv_mul_f32 v30, v15, v30\n"
      "\tv_mul_f32 v31, v15, v31\n"
      "\tv_mul_f32 v32, v15, v32\n"
      "\tv_mul_f32 v33, v15, v33\n"
      "\tv_mul_f32 v34, v15, v34\n"
      "\tv_mul_f32 v35, v15, v35\n"
      "\tv_mul_f32 v36, v15, v36\n"
      "\tv_mul_f32 v37, v15, v37\n"
      "\tv_mul_f32 v38, v15, v38\n"
      "\tv_mul_f32 v39, v15, v39\n"
      "\tv_mul_f32 v40, v15, v40\n"
      "\tv_mul_f32 v41, v15, v41\n"
      "\tv_mul_f32 v42, v15, v42\n"
      "\tv_mul_f32 v43, v15, v43\n"
      "\tv_mul_f32 v44, v15, v44\n"
      "\tv_mul_f32 v45, v15, v45\n"
      "\tv_mul_f32 v46, v15, v46\n"
      "\tv_mul_f32 v47, v15, v47\n"
      "\tv_mul_f32 v48, v15, v48\n"
      "\tv_mul_f32 v49, v15, v49\n"
      "\tv_mul_f32 v50, v15, v50\n"
      "\tv_mul_f32 v51, v15, v51\n"
      "\tv_mul_f32 v52, v15, v52\n"
      "\tv_mul_f32 v53, v15, v53\n"
      "\tv_mul_f32 v54, v15, v54\n"
      "\tv_mul_f32 v55, v15, v55\n"
      "\tv_mul_f32 v56, v15, v56\n"
      "\tv_mul_f32 v57, v15, v57\n"
      "\tv_mul_f32 v58, v15, v58\n"
      "\tv_mul_f32 v59, v15, v59\n"
      "\tv_mul_f32 v60, v15, v60\n"
      "\tv_mul_f32 v61, v15, v61\n"
      "\tv_mul_f32 v62, v15, v62\n"
      "\tv_mul_f32 v63, v15, v63\n"
      "\tv_mul_f32 v64, v15, v64\n"
      "\tv_mul_f32 v65, v15, v65\n"
      "\tv_mul_f32 v66, v15, v66\n"
      "\tv_mul_f32 v67, v15, v67\n"
      "\tv_mul_f32 v68, v15, v68\n"
      "\tv_mul_f32 v69, v15, v69\n"
      "\tv_mul_f32 v70, v15, v70\n"
      "\tv_mul_f32 v71, v15, v71\n"
      "\tv_mul_f32 v72, v15, v72\n"
      "\tv_mul_f32 v73, v15, v73\n"
      "\tv_mul_f32 v74, v15, v74\n"
      "\tv_mul_f32 v75, v15, v75\n"
      "\tv_mul_f32 v76, v15, v76\n"
      "\tv_mul_f32 v77, v15, v77\n"
      "\tv_mul_f32 v78, v15, v78\n"
      "\tv_mul_f32 v79, v15, v79\n"
      "\tv_mul_f32 v80, v15, v80\n"
      "\tv_mul_f32 v81, v15, v81\n"
      "\tv_mul_f32 v82, v15, v82\n"
      "\tv_mul_f32 v83, v15, v83\n"
      "\tv_mul_f32 v84, v15, v84\n"
      "\tv_mul_f32 v85, v15, v85\n"
      "\tv_mul_f32 v86, v15, v86\n"
      "\tv_mul_f32 v87, v15, v87\n"
      "\tv_mul_f32 v88, v15, v88\n"
      "\tv_mul_f32 v89, v15, v89\n"
      "\tv_mul_f32 v90, v15, v90\n"
      "\tv_mul_f32 v91, v15, v91\n"
      "\tv_mul_f32 v92, v15, v92\n"
      "\tv_mul_f32 v93, v15, v93\n"
      "\tv_mul_f32 v94, v15, v94\n"
      "\tv_mul_f32 v95, v15, v95\n"
      "\tv_mul_f32 v96, v15, v96\n"
      "\tv_mul_f32 v97, v15, v97\n"
      "\tv_mul_f32 v98, v15, v98\n"
      "\tv_mul_f32 v99, v15, v99\n"
      "\tv_mul_f32 v100, v15, v100\n"
      "\tv_mul_f32 v101, v15, v101\n"
      "\tv_mul_f32 v102, v15, v102\n"
      "\tv_mul_f32 v103, v15, v103\n"
      "\tv_mul_f32 v104, v15, v104\n"
      "\tv_mul_f32 v105, v15, v105\n"
      "\tv_mul_f32 v106, v15, v106\n"
      "\tv_mul_f32 v107, v15, v107\n"
      "\tv_mul_f32 v108, v15, v108\n"
      "\tv_mul_f32 v109, v15, v109\n"
      "\tv_mul_f32 v110, v15, v110\n"
      "\tv_mul_f32 v111, v15, v111\n"
      "\ts_nop 1\n"
      "\ts_setpc_b64 s[60:61]\n"
      "Lidle_%=:\n"
      "\ts_cmp_ge_u32 s59, 3\n"
      "\ts_cbranch_scc1 Ld2_%=\n"
      "\tds_read_b64_tr_b16 v[192:193], v6 offset:0\n"
      "\tds_read_b64_tr_b16 v[194:195], v6 offset:3072\n"
      "\tds_read_b64_tr_b16 v[196:197], v7 offset:0\n"
      "\tds_read_b64_tr_b16 v[198:199], v7 offset:3072\n"
      "\tds_read_b64_tr_b16 v[200:201], v6 offset:1024\n"
      "\tds_read_b64_tr_b16 v[202:203], v6 offset:4096\n"
      "\tds_read_b64_tr_b16 v[204:205], v7 offset:1024\n"
      "\tds_read_b64_tr_b16 v[206:207], v7 offset:4096\n"
      "\tds_read_b64_tr_b16 v[208:209], v6 offset:2048\n"
      "\tds_read_b64_tr_b16 v[210:211], v6 offset:5120\n"
      "\tds_read_b64_tr_b16 v[212:213], v7 offset:2048\n"
      "\tds_read_b64_tr_b16 v[214:215], v7 offset:5120\n"
      "\tds_read_b64_tr_b16 v[216:217], v6 offset:6144\n"
      "\tds_read_b64_tr_b16 v[218:219], v6 offset:9216\n"
      "\tds_read_b64_tr_b16 v[220:221], v7 offset:6144\n"
      "\tds_read_b64_tr_b16 v[222:223], v7 offset:9216\n"
      "\tds_read_b64_tr_b16 v[224:225], v6 offset:7168\n"
      "\tds_read_b64_tr_b16 v[226:227], v6 offset:10240\n"
      "\tds_read_b64_tr_b16 v[228:229], v7 offset:7168\n"
      "\tds_read_b64_tr_b16 v[230:231], v7 offset:10240\n"
      "\tds_read_b64_tr_b16 v[232:233], v6 offset:8192\n"
      "\tds_read_b64_tr_b16 v[234:235], v6 offset:11264\n"
      "\tds_read_b64_tr_b16 v[236:237], v7 offset:8192\n"
      "\tds_read_b64_tr_b16 v[238:239], v7 offset:11264\n"
      "\tv_mov_b32 v112, 0\n"
      "\tv_mov_b32 v113, 0\n"
      "\tv_mov_b32 v114, 0\n"
      "\tv_mov_b32 v115, 0\n"
      "\tv_mov_b32 v116, 0\n"
      "\tv_mov_b32 v117, 0\n"
      "\tv_mov_b32 v118, 0\n"
      "\tv_mov_b32 v119, 0\n"
      "\ts_branch Llast_%=\n"
      "Lone_%=:\n"
      "\tv_mov_b32 v176, v160\n"
      "\tv_mov_b32 v177, v161\n"
      "\tv_mov_b32 v178, v162\n"
      "\tv_mov_b32 v179, v163\n"
      "\tv_mov_b32 v180, v164\n"
      "\tv_mov_b32 v181, v165\n"
      "\tv_mov_b32 v182, v166\n"
      "\tv_mov_b32 v183, v167\n"
      "\tv_mov_b32 v184, v168\n"
      "\tv_mov_b32 v185, v169\n"
      "\tv_mov_b32 v186, v170\n"
      "\tv_mov_b32 v187, v171\n"
      "\tv_mov_b32 v188, v172\n"
      "\tv_mov_b32 v189, v173\n"
      "\tv_mov_b32 v190, v174\n"
      "\tv_mov_b32 v191, v175\n"
      "\tv_mov_b32 v170, 0\n"
      "\tv_mov_b32 v171, 0\n"
      "\tv_mov_b32 v172, 0\n"
      "\tv_mov_b32 v173, 0\n"
      "\tv_mov_b32 v174, 0\n"
      "\tv_mov_b32 v175, 0\n"
      "\tv_mov_b32 v240, 0\n"
      "\tv_mov_b32 v241, 0\n"
      "\tv_mov_b32 v242, 0\n"
      "\tv_mov_b32 v243, 0\n"
      "\tv_mov_b32 v244, 0\n"
      "\tv_mov_b32 v245, 0\n"
      "\tv_mov_b32 v246, 0\n"
      "\tv_mov_b32 v247, 0\n"
      "\tv_subrev_u32 v6, 12288, v6\n"
      "\tv_subrev_u32 v7, 12288, v7\n"
      "\ts_mov_b32 s85, 0\n"
      "\ts_branch Lbf0_%=\n"
      "Ld1_%=:\n"
      "\ts_call_b64 s[60:61], Llk_%=\n"
      "\ts_call_b64 s[60:61], Llv_%=\n"
      "\ts_call_b64 s[60:61], Llk_%=\n"
      "\ts_call_b64 s[60:61], Llv_%=\n"
      "Ld2_%=:\n"
      "\ts_cmp_eq_u32 s88, 0\n"
      "\ts_cbranch_scc1 Lhelp_%=\n"
      "\ts_sub_u32 s88, s88, 1\n"
      "\ts_bitcmp0_b32 s56, 4\n"
      "\ts_cbranch_scc1 Ld4_%=\n"
      "\ts_waitcnt vmcnt(18)\n"
      "\ts_branch Ld5_%=\n"
      "Ld4_%=:\n"
      "\ts_bitcmp0_b32 s56, 0\n"
      "\ts_waitcnt vmcnt(12)\n"
      "\ts_cbranch_scc1 1f\n"
      "\ts_waitcnt vmcnt(9)\n"
      "1:\n"
      "Ld5_%=:\n"
      "\ts_cmp_lg_u32 s81, 0\n"
      "\ts_barrier\n"
      "\ts_cbranch_scc1 Ld1_%=\n"
      "\ts_branch Ld2_%=\n"
      "Lbf0_%=:\n"
      "\tv_mfma_f32_32x32x16_bf16 v[16:31], v[192:195], v[240:243], v[16:31]\n"
      "\tv_mov_b32 v120, 0xff800000\n"
      "\tv_add_f32 v11, v11, v170\n"
      "\tv_add_f32 v11, v11, v171\n"
      "\tv_add_f32 v11, v11, v172\n"
      "\tv_add_f32 v11, v11, v173\n"
      "\tv_add_f32 v11, v11, v174\n"
      "\tv_add_f32 v11, v11, v175\n"
      "\tv_cmp_lt_i32_e64 vcc, 0, v1\n"
      "\tv_cmp_lt_i32_e64 s[64:65], 1, v1\n"
      "\ts_nop 0\n"
      "\tv_cmp_lt_i32_e64 s[66:67], 2, v1\n"
      "\tv_cndmask_b32_e64 v176, v120, v176, vcc\n"
      "\tv_cmp_lt_i32_e64 vcc, 3, v1\n"
      "\tv_mfma_f32_32x32x16_bf16 v[32:47], v[196:199], v[240:243], v[32:47]\n"
      "\tv_cndmask_b32_e64 v177, v120, v177, s[64:65]\n"
      "\tv_cmp_lt_i32_e64 s[64:65], 8, v1\n"
      "\tv_cndmask_b32_e64 v178, v120, v178, s[66:67]\n"
      "\tv_cmp_lt_i32_e64 s[66:67], 9, v1\n"
      "\tv_cndmask_b32_e64 v179, v120, v179, vcc\n"
      "\tv_cmp_lt_i32_e64 vcc, 10, v1\n"
      "\tv_cndmask_b32_e64 v180, v120, v180, s[64:65]\n"
      "\tv_cmp_lt_i32_e64 s[64:65], 11, v1\n"
      "\tv_cndmask_b32_e64 v181, v120, v181, s[66:67]\n"
      "\tv_cmp_lt_i32_e64 s[66:67], 16, v1\n"
      "\tv_cndmask_b32_e64 v182, v120, v182, vcc\n"
      "\tv_cmp_lt_i32_e64 vcc, 17, v1\n"
      "\tv_cndmask_b32_e64 v183, v120, v183, s[64:65]\n"
      "\tv_mfma_f32_32x32x16_bf16 v[48:63], v[200:203], v[240:243], v[48:63]\n"
      "\tv_cmp_lt_i32_e64 s[64:65], 18, v1\n"
      "\tv_cndmask_b32_e64 v184, v120, v184, s[66:67]\n"
      "\tv_cmp_lt_i32_e64 s[66:67], 19, v1\n"
      "\tv_cndmask_b32_e64 v185, v120, v185, vcc\n"
      "\tv_cmp_lt_i32_e64 vcc, 24, v1\n"
      "\tv_cndmask_b32_e64 v186, v120, v186, s[64:65]\n"
      "\tv_cmp_lt_i32_e64 s[64:65], 25, v1\n"
      "\tv_cndmask_b32_e64 v187, v120, v187, s[66:67]\n"
      "\tv_cmp_lt_i32_e64 s[66:67], 26, v1\n"
      "\tv_cndmask_b32_e64 v188, v120, v188, vcc\n"
      "\tv_cmp_lt_i32_e64 vcc, 27, v1\n"
      "\tv_cndmask_b32_e64 v189, v120, v189, s[64:65]\n"
      "\ts_nop 0\n"
      "\tv_mfma_f32_32x32x16_bf16 v[64:79], v[204:207], v[240:243], v[64:79]\n"
      "\tv_cndmask_b32_e64 v190, v120, v190, s[66:67]\n"
      "\ts_nop 0\n"
      "\tv_cndmask_b32_e64 v191, v120, v191, vcc\n"
      "\tv_max3_f32 v13, v176, v177, v178\n"
      "\tv_max3_f32 v14, v179, v180, v181\n"
      "\tv_max3_f32 v13, v13, v182, v183\n"
      "\tv_max3_f32 v14, v14, v184, v185\n"
      "\tv_max3_f32 v13, v13, v186, v187\n"
      "\ts_cmp_lg_u32 s85, 0\n"
      "\ts_waitcnt lgkmcnt(0)\n"
      "\ts_cbranch_scc1 Lsy0f_%=\n"
      "Lsr0f_%=:\n"
      "\tv_mfma_f32_32x32x16_bf16 v[80:95], v[208:211], v[240:243], v[80:95]\n"
      "\tds_read_b64_tr_b16 v[192:193], v6 offset:12288\n"
      "\tds_read_b64_tr_b16 v[194:195], v6 offset:15360\n"
      "\tds_read_b64_tr_b16 v[196:197], v7 offset:12288\n"
      "\tv_max3_f32 v14, v14, v188, v189\n"
      "\tv_max3_f32 v13, v13, v190, v191\n"
      "\tv_max_f32 v13, v13, v14\n"
      "\tv_cmp_nle_f32 vcc, v13, v12\n"
      "\ts_nop 0\n"
      "\ts_cbranch_vccnz Lrs0f_%=\n"
      "Lrr0f_%=:\n"
      "\tv_fma_f32 v176, v176, s93, v10\n"
      "\tv_fma_f32 v177, v177, s93, v10\n"
      "\tv_exp_f32 v176, v176\n"
      "\tv_mfma_f32_32x32x16_bf16 v[96:111], v[212:215], v[240:243], v[96:111]\n"
      "\tds_read_b64_tr_b16 v[198:199], v7 offset:15360\n"
      "\tds_read_b64_tr_b16 v[200:201], v6 offset:13312\n"
      "\tds_read_b64_tr_b16 v[202:203], v6 offset:16384\n"
      "\tv_fma_f32 v178, v178, s93, v10\n"
      "\tv_exp_f32 v177, v177\n"
      "\tv_add_f32 v11, v11, v176\n"
      "\tv_fma_f32 v179, v179, s93, v10\n"
      "\tv_exp_f32 v178, v178\n"
      "\tv_add_f32 v11, v11, v177\n"
      "\tv_cvt_pk_bf16_f32 v112, v176, v177\n"
      "\tv_fma_f32 v180, v180, s93, v10\n"
      "\tv_mfma_f32_32x32x16_bf16 v[16:31], v[216:219], v[244:247], v[16:31]\n"
      "\tds_read_b64_tr_b16 v[204:205], v7 offset:13312\n"
      "\tds_read_b64_tr_b16 v[206:207], v7 offset:16384\n"
      "\tds_read_b64_tr_b16 v[208:209], v6 offset:14336\n"
      "\tv_exp_f32 v179, v179\n"
      "\tv_add_f32 v11, v11, v178\n"
      "\tv_fma_f32 v181, v181, s93, v10\n"
      "\tv_exp_f32 v180, v180\n"
      "\tv_add_f32 v11, v11, v179\n"
      "\tv_cvt_pk_bf16_f32 v113, v178, v179\n"
      "\tv_fma_f32 v182, v182, s93, v10\n"
      "\tv_mfma_f32_32x32x16_bf16 v[32:47], v[220:223], v[244:247], v[32:47]\n"
      "\tds_read_b64_tr_b16 v[210:211], v6 offset:17408\n"
      "\tds_read_b64_tr_b16 v[212:213], v7 offset:14336\n"
      "\tds_read_b64_tr_b16 v[214:215], v7 offset:17408\n"
      "\tv_exp_f32 v181, v181\n"
      "\tv_add_f32 v11, v11, v180\n"
      "\tv_fma_f32 v183, v183, s93, v10\n"
      "\tv_exp_f32 v182, v182\n"
      "\tv_add_f32 v11, v11, v181\n"
      "\tv_cvt_pk_bf16_f32 v114, v180, v181\n"
      "\tv_fma_f32 v184, v184, s93, v10\n"
      "\tv_exp_f32 v183, v183\n"
      "\tv_mfma_f32_32x32x16_bf16 v[48:63], v[224:227], v[244:247], v[48:63]\n"
      "\tds_read_b64_tr_b16 v[216:217], v6 offset:18432\n"
      "\tds_read_b64_tr_b16 v[218:219], v6 offset:21504\n"
      "\tds_read_b64_tr_b16 v[220:221], v7 offset:18432\n"
      "\tv_add_f32 v11, v11, v182\n"
      "\tv_fma_f32 v185, v185, s93, v10\n"
      "\tv_exp_f32 v184, v184\n"
      "\tv_add_f32 v11, v11, v183\n"
      "\tv_cvt_pk_bf16_f32 v115, v182, v183\n"
      "\tv_fma_f32 v186, v186, s93, v10\n"
      "\tv_exp_f32 v185, v185\n"
      "\tv_add_f32 v11, v11, v184\n"
      "\tv_mfma_f32_32x32x16_bf16 v[64:79], v[228:231], v[244:247], v[64:79]\n"
      "\tds_read_b64_tr_b16 v[222:223], v7 offset:21504\n"
      "\tds_read_b64_tr_b16 v[224:225], v6 offset:19456\n"
      "\tds_read_b64_tr_b16 v[226:227], v6 offset:22528\n"
      "\tv_fma_f32 v187, v187, s93, v10\n"
      "\tv_exp_f32 v186, v186\n"
      "\tv_add_f32 v11, v11, v185\n"
      "\tv_cvt_pk_bf16_f32 v116, v184, v185\n"
      "\tv_fma_f32 v188, v188, s93, v10\n"
      "\tv_exp_f32 v187, v187\n"
      "\tv_add_f32 v11, v11, v186\n"
      "\tv_mfma_f32_32x32x16_bf16 v[80:95], v[232:235], v[244:247], v[80:95]\n"
      "\tds_read_b64_tr_b16 v[228:229], v7 offset:19456\n"
      "\tds_read_b64_tr_b16 v[230:231], v7 offset:22528\n"
      "\tds_read_b64_tr_b16 v[232:233], v6 offset:20480\n"
      "\tv_fma_f32 v189, v189, s93, v10\n"
      "\tv_exp_f32 v188, v188\n"
      "\tv_add_f32 v11, v11, v187\n"
      "\tv_cvt_pk_bf16_f32 v117, v186, v187\n"
      "\tv_fma_f32 v190, v190, s93, v10\n"
      "\tv_exp_f32 v189, v189\n"
      "\tv_add_f32 v11, v11, v188\n"
      "\tv_fma_f32 v191, v191, s93, v10\n"
      "\tv_mfma_f32_32x32x16_bf16 v[96:111], v[236:239], v[244:247], v[96:111]\n"
      "\tds_read_b64_tr_b16 v[234:235], v6 offset:23552\n"
      "\tds_read_b64_tr_b16 v[236:237], v7 offset:20480\n"
      "\tds_read_b64_tr_b16 v[238:239], v7 offset:23552\n"
      "\tv_exp_f32 v190, v190\n"
      "\tv_add_f32 v11, v11, v189\n"
      "\tv_cvt_pk_bf16_f32 v118, v188, v189\n"
      "\tv_exp_f32 v191, v191\n"
      "\tv_add_f32 v11, v11, v190\n"
      "\ts_cmp_lg_u32 s95, 0\n"
      "\tv_add_f32 v11, v11, v191\n"
      "\tv_cvt_pk_bf16_f32 v119, v190, v191\n"
      "\ts_cbranch_scc0 Llast_%=\n"
      "\ts_nop 7\n"
      "\ts_call_b64 s[60:61], Lscale_%=\n"
      "\ts_branch Llast_%=\n"
      "Lbf1_%=:\n"
      "\tv_mfma_f32_32x32x16_bf16 v[16:31], v[192:195], v[240:243], v[16:31]\n"
      "\tv_mov_b32 v120, 0xff800000\n"
      "\ts_cmp_eq_u32 s62, 49152\n"
      "\ts_cselect_b32 s96, s68, s63\n"
      "\ts_add_u32 s62, s62, s96\n"
      "\tv_add_u32 v6, s96, v6\n"
      "\tv_add_u32 v7, s96, v7\n"
      "\tv_add_f32 v11, v11, v186\n"
      "\tv_add_f32 v11, v11, v187\n"
      "\tv_add_f32 v11, v11, v188\n"
      "\tv_add_f32 v11, v11, v189\n"
      "\tv_add_f32 v11, v11, v190\n"
      "\tv_add_f32 v11, v11, v191\n"
      "\tv_cmp_lt_i32_e64 vcc, 0, v1\n"
      "\tv_mfma_f32_32x32x16_bf16 v[32:47], v[196:199], v[240:243], v[32:47]\n"
      "\tv_cmp_lt_i32_e64 s[64:65], 1, v1\n"
      "\ts_nop 0\n"
      "\tv_cmp_lt_i32_e64 s[66:67], 2, v1\n"
      "\tv_cndmask_b32_e64 v160, v120, v160, vcc\n"
      "\tv_cmp_lt_i32_e64 vcc, 3, v1\n"
      "\tv_cndmask_b32_e64 v161, v120, v161, s[64:65]\n"
      "\tv_cmp_lt_i32_e64 s[64:65], 8, v1\n"
      "\tv_cndmask_b32_e64 v162, v120, v162, s[66:67]\n"
      "\tv_cmp_lt_i32_e64 s[66:67], 9, v1\n"
      "\tv_cndmask_b32_e64 v163, v120, v163, vcc\n"
      "\tv_cmp_lt_i32_e64 vcc, 10, v1\n"
      "\tv_cndmask_b32_e64 v164, v120, v164, s[64:65]\n"
      "\tv_cmp_lt_i32_e64 s[64:65], 11, v1\n"
      "\tv_cndmask_b32_e64 v165, v120, v165, s[66:67]\n"
      "\tv_mfma_f32_32x32x16_bf16 v[48:63], v[200:203], v[240:243], v[48:63]\n"
      "\tv_cmp_lt_i32_e64 s[66:67], 16, v1\n"
      "\tv_cndmask_b32_e64 v166, v120, v166, vcc\n"
      "\tv_cmp_lt_i32_e64 vcc, 17, v1\n"
      "\tv_cndmask_b32_e64 v167, v120, v167, s[64:65]\n"
      "\tv_cmp_lt_i32_e64 s[64:65], 18, v1\n"
      "\tv_cndmask_b32_e64 v168, v120, v168, s[66:67]\n"
      "\tv_cmp_lt_i32_e64 s[66:67], 19, v1\n"
      "\tv_cndmask_b32_e64 v169, v120, v169, vcc\n"
      "\tv_cmp_lt_i32_e64 vcc, 24, v1\n"
      "\tv_cndmask_b32_e64 v170, v120, v170, s[64:65]\n"
      "\tv_cmp_lt_i32_e64 s[64:65], 25, v1\n"
      "\tv_cndmask_b32_e64 v171, v120, v171, s[66:67]\n"
      "\tv_cmp_lt_i32_e64 s[66:67], 26, v1\n"
      "\tv_mfma_f32_32x32x16_bf16 v[64:79], v[204:207], v[240:243], v[64:79]\n"
      "\tv_cndmask_b32_e64 v172, v120, v172, vcc\n"
      "\tv_cmp_lt_i32_e64 vcc, 27, v1\n"
      "\tv_cndmask_b32_e64 v173, v120, v173, s[64:65]\n"
      "\ts_nop 0\n"
      "\tv_cndmask_b32_e64 v174, v120, v174, s[66:67]\n"
      "\ts_nop 0\n"
      "\tv_cndmask_b32_e64 v175, v120, v175, vcc\n"
      "\tv_max3_f32 v13, v160, v161, v162\n"
      "\tv_max3_f32 v14, v163, v164, v165\n"
      "\ts_cmp_lg_u32 s69, 0\n"
      "\ts_waitcnt lgkmcnt(0)\n"
      "\ts_cbranch_scc1 Lsy1f_%=\n"
      "\ts_barrier\n"
      "Lsr1f_%=:\n"
      "\tv_mfma_f32_32x32x16_bf16 v[80:95], v[208:211], v[240:243], v[80:95]\n"
      "\tds_read_b64_tr_b16 v[192:193], v6 offset:0\n"
      "\tds_read_b64_tr_b16 v[194:195], v6 offset:3072\n"
      "\tds_read_b64_tr_b16 v[196:197], v7 offset:0\n"
      "\tv_max3_f32 v13, v13, v166, v167\n"
      "\tv_max3_f32 v14, v14, v168, v169\n"
      "\tv_max3_f32 v13, v13, v170, v171\n"
      "\tv_max3_f32 v14, v14, v172, v173\n"
      "\tv_max3_f32 v13, v13, v174, v175\n"
      "\tv_max_f32 v13, v13, v14\n"
      "\tv_cmp_nle_f32 vcc, v13, v12\n"
      "\ts_nop 0\n"
      "\ts_cbranch_vccnz Lrs1f_%=\n"
      "Lrr1f_%=:\n"
      "\tv_fma_f32 v160, v160, s93, v10\n"
      "\tv_mfma_f32_32x32x16_bf16 v[96:111], v[212:215], v[240:243], v[96:111]\n"
      "\tds_read_b64_tr_b16 v[198:199], v7 offset:3072\n"
      "\tds_read_b64_tr_b16 v[200:201], v6 offset:1024\n"
      "\tds_read_b64_tr_b16 v[202:203], v6 offset:4096\n"
      "\tv_fma_f32 v161, v161, s93, v10\n"
      "\tv_exp_f32 v160, v160\n"
      "\tv_fma_f32 v162, v162, s93, v10\n"
      "\tv_exp_f32 v161, v161\n"
      "\tv_add_f32 v11, v11, v160\n"
      "\tv_fma_f32 v163, v163, s93, v10\n"
      "\tv_exp_f32 v162, v162\n"
      "\tv_add_f32 v11, v11, v161\n"
      "\tv_mfma_f32_32x32x16_bf16 v[16:31], v[216:219], v[244:247], v[16:31]\n"
      "\tds_read_b64_tr_b16 v[204:205], v7 offset:1024\n"
      "\tds_read_b64_tr_b16 v[206:207], v7 offset:4096\n"
      "\tds_read_b64_tr_b16 v[208:209], v6 offset:2048\n"
      "\tv_cvt_pk_bf16_f32 v112, v160, v161\n"
      "\tv_fma_f32 v164, v164, s93, v10\n"
      "\tv_exp_f32 v163, v163\n"
      "\tv_add_f32 v11, v11, v162\n"
      "\tv_fma_f32 v165, v165, s93, v10\n"
      "\tv_exp_f32 v164, v164\n"
      "\tv_add_f32 v11, v11, v163\n"
      "\tv_cvt_pk_bf16_f32 v113, v162, v163\n"
      "\tv_mfma_f32_32x32x16_bf16 v[32:47], v[220:223], v[244:247], v[32:47]\n"
      "\tds_read_b64_tr_b16 v[210:211], v6 offset:5120\n"
      "\tds_read_b64_tr_b16 v[212:213], v7 offset:2048\n"
      "\tds_read_b64_tr_b16 v[214:215], v7 offset:5120\n"
      "\tv_fma_f32 v166, v166, s93, v10\n"
      "\tv_exp_f32 v165, v165\n"
      "\tv_add_f32 v11, v11, v164\n"
      "\tv_fma_f32 v167, v167, s93, v10\n"
      "\tv_exp_f32 v166, v166\n"
      "\tv_add_f32 v11, v11, v165\n"
      "\tv_cvt_pk_bf16_f32 v114, v164, v165\n"
      "\tv_fma_f32 v168, v168, s93, v10\n"
      "\tv_mfma_f32_32x32x16_bf16 v[48:63], v[224:227], v[244:247], v[48:63]\n"
      "\tds_read_b64_tr_b16 v[216:217], v6 offset:6144\n"
      "\tds_read_b64_tr_b16 v[218:219], v6 offset:9216\n"
      "\tds_read_b64_tr_b16 v[220:221], v7 offset:6144\n"
      "\tv_exp_f32 v167, v167\n"
      "\tv_add_f32 v11, v11, v166\n"
      "\tv_fma_f32 v169, v169, s93, v10\n"
      "\tv_exp_f32 v168, v168\n"
      "\tv_add_f32 v11, v11, v167\n"
      "\tv_cvt_pk_bf16_f32 v115, v166, v167\n"
      "\tv_fma_f32 v170, v170, s93, v10\n"
      "\tv_exp_f32 v169, v169\n"
      "\tv_mfma_f32_32x32x16_bf16 v[64:79], v[228:231], v[244:247], v[64:79]\n"
      "\tds_read_b64_tr_b16 v[222:223], v7 offset:9216\n"
      "\tds_read_b64_tr_b16 v[224:225], v6 offset:7168\n"
      "\tds_read_b64_tr_b16 v[226:227], v6 offset:10240\n"
      "\tv_add_f32 v11, v11, v168\n"
      "\tv_fma_f32 v171, v171, s93, v10\n"
      "\tv_exp_f32 v170, v170\n"
      "\tv_add_f32 v11, v11, v169\n"
      "\tv_cvt_pk_bf16_f32 v116, v168, v169\n"
      "\tv_fma_f32 v172, v172, s93, v10\n"
      "\tv_exp_f32 v171, v171\n"
      "\tv_add_f32 v11, v11, v170\n"
      "\tv_mfma_f32_32x32x16_bf16 v[80:95], v[232:235], v[244:247], v[80:95]\n"
      "\tds_read_b64_tr_b16 v[228:229], v7 offset:7168\n"
      "\tds_read_b64_tr_b16 v[230:231], v7 offset:10240\n"
      "\tds_read_b64_tr_b16 v[232:233], v6 offset:8192\n"
      "\tv_fma_f32 v173, v173, s93, v10\n"
      "\tv_exp_f32 v172, v172\n"
      "\tv_add_f32 v11, v11, v171\n"
      "\tv_cvt_pk_bf16_f32 v117, v170, v171\n"
      "\tv_fma_f32 v174, v174, s93, v10\n"
      "\tv_exp_f32 v173, v173\n"
      "\tv_add_f32 v11, v11, v172\n"
      "\tv_fma_f32 v175, v175, s93, v10\n"
      "\tv_mfma_f32_32x32x16_bf16 v[96:111], v[236:239], v[244:247], v[96:111]\n"
      "\tds_read_b64_tr_b16 v[234:235], v6 offset:11264\n"
      "\tds_read_b64_tr_b16 v[236:237], v7 offset:8192\n"
      "\tds_read_b64_tr_b16 v[238:239], v7 offset:11264\n"
      "\tv_exp_f32 v174, v174\n"
      "\tv_add_f32 v11, v11, v173\n"
      "\tv_cvt_pk_bf16_f32 v118, v172, v173\n"
      "\tv_exp_f32 v175, v175\n"
      "\tv_add_f32 v11, v11, v174\n"
      "\ts_cmp_lg_u32 s95, 0\n"
      "\tv_add_f32 v11, v11, v175\n"
      "\tv_cvt_pk_bf16_f32 v119, v174, v175\n"
      "\ts_cbranch_scc0 Llast_%=\n"
      "\ts_nop 7\n"
      "\ts_call_b64 s[60:61], Lscale_%=\n"
      "Llast_%=:\n"
      "\ts_cmp_eq_u32 s88, 0\n"
      "\ts_cbranch_scc1 Ll1_%=\n"
      "\ts_waitcnt vmcnt(0) lgkmcnt(0)\n"
      "\ts_barrier\n"
      "Ll1_%=:\n"
      "\tv_mov_b32 v13, v11\n"
      "\ts_waitcnt lgkmcnt(8)\n"
      "\tv_mfma_f32_32x32x16_bf16 v[16:31], v[192:195], v[112:115], v[16:31]\n"
      "\tv_mbcnt_lo_u32_b32 v2, -1, 0\n"
      "\tv_permlane32_swap_b32 v11, v13\n"
      "\tv_mbcnt_hi_u32_b32 v2, -1, v2\n"
      "\tv_mfma_f32_32x32x16_bf16 v[16:31], v[216:219], v[116:119], v[16:31]\n"
      "\tv_add_f32 v11, v11, v13\n"
      "\tv_mov_b32 v176, s98\n"
      "\tv_mov_b32 v177, 1\n"
      "\tv_rcp_f32 v12, v11\n"
      "\tv_mfma_f32_32x32x16_bf16 v[32:47], v[196:199], v[112:115], v[32:47]\n"
      "\tv_mov_b32 v178, 2\n"
      "\tv_mov_b32 v179, 3\n"
      "\ts_bitcmp1_b32 0x6, s59\n"
      "\ts_waitcnt lgkmcnt(0)\n"
      "\tv_mfma_f32_32x32x16_bf16 v[32:47], v[220:223], v[116:119], v[32:47]\n"
      "\ts_cbranch_scc1 Lpair_%=\n"
      "\tv_mfma_f32_32x32x16_bf16 v[48:63], v[200:203], v[112:115], v[48:63]\n"
      "\tv_mul_f32 v16, v12, v16\n"
      "\tv_mul_f32 v17, v12, v17\n"
      "\tv_mul_f32 v18, v12, v18\n"
      "\tv_mul_f32 v19, v12, v19\n"
      "\tv_cvt_pk_bf16_f32 v16, v16, v17\n"
      "\tv_cvt_pk_bf16_f32 v17, v18, v19\n"
      "\tds_write_b64 v248, v[16:17] offset:0\n"
      "\tv_mfma_f32_32x32x16_bf16 v[48:63], v[224:227], v[116:119], v[48:63]\n"
      "\tv_mul_f32 v20, v12, v20\n"
      "\tv_mul_f32 v21, v12, v21\n"
      "\tv_mul_f32 v22, v12, v22\n"
      "\tv_mul_f32 v23, v12, v23\n"
      "\tv_cvt_pk_bf16_f32 v20, v20, v21\n"
      "\tv_cvt_pk_bf16_f32 v21, v22, v23\n"
      "\tds_write_b64 v249, v[20:21] offset:0\n"
      "\tv_mfma_f32_32x32x16_bf16 v[64:79], v[204:207], v[112:115], v[64:79]\n"
      "\tv_mul_f32 v24, v12, v24\n"
      "\tv_mul_f32 v25, v12, v25\n"
      "\tv_mul_f32 v26, v12, v26\n"
      "\tv_mul_f32 v27, v12, v27\n"
      "\tv_cvt_pk_bf16_f32 v24, v24, v25\n"
      "\tv_cvt_pk_bf16_f32 v25, v26, v27\n"
      "\tds_write_b64 v250, v[24:25] offset:0\n"
      "\tv_mfma_f32_32x32x16_bf16 v[64:79], v[228:231], v[116:119], v[64:79]\n"
      "\tv_mul_f32 v28, v12, v28\n"
      "\tv_mul_f32 v29, v12, v29\n"
      "\tv_mul_f32 v30, v12, v30\n"
      "\tv_mul_f32 v31, v12, v31\n"
      "\tv_cvt_pk_bf16_f32 v28, v28, v29\n"
      "\tv_cvt_pk_bf16_f32 v29, v30, v31\n"
      "\tds_write_b64 v251, v[28:29] offset:0\n"
      "\tv_mfma_f32_32x32x16_bf16 v[80:95], v[208:211], v[112:115], v[80:95]\n"
      "\tv_mul_f32 v32, v12, v32\n"
      "\tv_mul_f32 v33, v12, v33\n"
      "\tv_mul_f32 v34, v12, v34\n"
      "\tv_mul_f32 v35, v12, v35\n"
      "\tv_cvt_pk_bf16_f32 v32, v32, v33\n"
      "\tv_cvt_pk_bf16_f32 v33, v34, v35\n"
      "\tds_write_b64 v252, v[32:33] offset:0\n"
      "\tv_mfma_f32_32x32x16_bf16 v[80:95], v[232:235], v[116:119], v[80:95]\n"
      "\tv_mul_f32 v36, v12, v36\n"
      "\tv_mul_f32 v37, v12, v37\n"
      "\tv_mul_f32 v38, v12, v38\n"
      "\tv_mul_f32 v39, v12, v39\n"
      "\tv_cvt_pk_bf16_f32 v36, v36, v37\n"
      "\tv_cvt_pk_bf16_f32 v37, v38, v39\n"
      "\tds_write_b64 v253, v[36:37] offset:0\n"
      "\tv_mfma_f32_32x32x16_bf16 v[96:111], v[212:215], v[112:115], v[96:111]\n"
      "\tv_mul_f32 v40, v12, v40\n"
      "\tv_mul_f32 v41, v12, v41\n"
      "\tv_mul_f32 v42, v12, v42\n"
      "\tv_mul_f32 v43, v12, v43\n"
      "\tv_cvt_pk_bf16_f32 v40, v40, v41\n"
      "\tv_cvt_pk_bf16_f32 v41, v42, v43\n"
      "\tds_write_b64 v254, v[40:41] offset:0\n"
      "\tv_mfma_f32_32x32x16_bf16 v[96:111], v[236:239], v[116:119], v[96:111]\n"
      "\tv_mul_f32 v44, v12, v44\n"
      "\tv_mul_f32 v45, v12, v45\n"
      "\tv_mul_f32 v46, v12, v46\n"
      "\tv_mul_f32 v47, v12, v47\n"
      "\tv_cvt_pk_bf16_f32 v44, v44, v45\n"
      "\tv_cvt_pk_bf16_f32 v45, v46, v47\n"
      "\tds_write_b64 v255, v[44:45] offset:0\n"
      "\ts_cmp_eq_u32 s59, 0\n"
      "\ts_cbranch_scc1 Lself_%=\n"
      "\tds_write_b32 v176, v177 offset:256\n"
      "\tv_pk_mul_f32 v[48:49], v[48:49], v[12:13] op_sel_hi:[1,0]\n"
      "\tv_pk_mul_f32 v[50:51], v[50:51], v[12:13] op_sel_hi:[1,0]\n"
      "\tv_cvt_pk_bf16_f32 v48, v48, v49\n"
      "\tv_cvt_pk_bf16_f32 v49, v50, v51\n"
      "\tds_write_b64 v248, v[48:49] offset:4096\n"
      "\tv_pk_mul_f32 v[52:53], v[52:53], v[12:13] op_sel_hi:[1,0]\n"
      "\tv_pk_mul_f32 v[54:55], v[54:55], v[12:13] op_sel_hi:[1,0]\n"
      "\tv_cvt_pk_bf16_f32 v52, v52, v53\n"
      "\tv_cvt_pk_bf16_f32 v53, v54, v55\n"
      "\tds_write_b64 v249, v[52:53] offset:4096\n"
      "\tv_pk_mul_f32 v[56:57], v[56:57], v[12:13] op_sel_hi:[1,0]\n"
      "\tv_pk_mul_f32 v[58:59], v[58:59], v[12:13] op_sel_hi:[1,0]\n"
      "\tv_cvt_pk_bf16_f32 v56, v56, v57\n"
      "\tv_cvt_pk_bf16_f32 v57, v58, v59\n"
      "\tds_write_b64 v250, v[56:57] offset:4096\n"
      "\tv_pk_mul_f32 v[60:61], v[60:61], v[12:13] op_sel_hi:[1,0]\n"
      "\tv_pk_mul_f32 v[62:63], v[62:63], v[12:13] op_sel_hi:[1,0]\n"
      "\tv_cvt_pk_bf16_f32 v60, v60, v61\n"
      "\tv_cvt_pk_bf16_f32 v61, v62, v63\n"
      "\tds_write_b64 v251, v[60:61] offset:4096\n"
      "\tv_pk_mul_f32 v[64:65], v[64:65], v[12:13] op_sel_hi:[1,0]\n"
      "\tv_pk_mul_f32 v[66:67], v[66:67], v[12:13] op_sel_hi:[1,0]\n"
      "\tv_cvt_pk_bf16_f32 v64, v64, v65\n"
      "\tv_cvt_pk_bf16_f32 v65, v66, v67\n"
      "\tds_write_b64 v252, v[64:65] offset:4096\n"
      "\tv_pk_mul_f32 v[68:69], v[68:69], v[12:13] op_sel_hi:[1,0]\n"
      "\tv_pk_mul_f32 v[70:71], v[70:71], v[12:13] op_sel_hi:[1,0]\n"
      "\tv_cvt_pk_bf16_f32 v68, v68, v69\n"
      "\tv_cvt_pk_bf16_f32 v69, v70, v71\n"
      "\tds_write_b64 v253, v[68:69] offset:4096\n"
      "\tv_pk_mul_f32 v[72:73], v[72:73], v[12:13] op_sel_hi:[1,0]\n"
      "\tv_pk_mul_f32 v[74:75], v[74:75], v[12:13] op_sel_hi:[1,0]\n"
      "\tv_cvt_pk_bf16_f32 v72, v72, v73\n"
      "\tv_cvt_pk_bf16_f32 v73, v74, v75\n"
      "\tds_write_b64 v254, v[72:73] offset:4096\n"
      "\tv_pk_mul_f32 v[76:77], v[76:77], v[12:13] op_sel_hi:[1,0]\n"
      "\tv_pk_mul_f32 v[78:79], v[78:79], v[12:13] op_sel_hi:[1,0]\n"
      "\tv_cvt_pk_bf16_f32 v76, v76, v77\n"
      "\tv_cvt_pk_bf16_f32 v77, v78, v79\n"
      "\tds_write_b64 v255, v[76:77] offset:4096\n"
      "\tds_write_b32 v176, v178 offset:256\n"
      "\tv_pk_mul_f32 v[80:81], v[80:81], v[12:13] op_sel_hi:[1,0]\n"
      "\tv_pk_mul_f32 v[82:83], v[82:83], v[12:13] op_sel_hi:[1,0]\n"
      "\tv_cvt_pk_bf16_f32 v80, v80, v81\n"
      "\tv_cvt_pk_bf16_f32 v81, v82, v83\n"
      "\tds_write_b64 v248, v[80:81] offset:8192\n"
      "\tv_pk_mul_f32 v[84:85], v[84:85], v[12:13] op_sel_hi:[1,0]\n"
      "\tv_pk_mul_f32 v[86:87], v[86:87], v[12:13] op_sel_hi:[1,0]\n"
      "\tv_cvt_pk_bf16_f32 v84, v84, v85\n"
      "\tv_cvt_pk_bf16_f32 v85, v86, v87\n"
      "\tds_write_b64 v249, v[84:85] offset:8192\n"
      "\tv_pk_mul_f32 v[88:89], v[88:89], v[12:13] op_sel_hi:[1,0]\n"
      "\tv_pk_mul_f32 v[90:91], v[90:91], v[12:13] op_sel_hi:[1,0]\n"
      "\tv_cvt_pk_bf16_f32 v88, v88, v89\n"
      "\tv_cvt_pk_bf16_f32 v89, v90, v91\n"
      "\tds_write_b64 v250, v[88:89] offset:8192\n"
      "\tv_pk_mul_f32 v[92:93], v[92:93], v[12:13] op_sel_hi:[1,0]\n"
      "\tv_pk_mul_f32 v[94:95], v[94:95], v[12:13] op_sel_hi:[1,0]\n"
      "\tv_cvt_pk_bf16_f32 v92, v92, v93\n"
      "\tv_cvt_pk_bf16_f32 v93, v94, v95\n"
      "\tds_write_b64 v251, v[92:93] offset:8192\n"
      "\tv_pk_mul_f32 v[96:97], v[96:97], v[12:13] op_sel_hi:[1,0]\n"
      "\tv_pk_mul_f32 v[98:99], v[98:99], v[12:13] op_sel_hi:[1,0]\n"
      "\tv_cvt_pk_bf16_f32 v96, v96, v97\n"
      "\tv_cvt_pk_bf16_f32 v97, v98, v99\n"
      "\tds_write_b64 v252, v[96:97] offset:8192\n"
      "\tv_pk_mul_f32 v[100:101], v[100:101], v[12:13] op_sel_hi:[1,0]\n"
      "\tv_pk_mul_f32 v[102:103], v[102:103], v[12:13] op_sel_hi:[1,0]\n"
      "\tv_cvt_pk_bf16_f32 v100, v100, v101\n"
      "\tv_cvt_pk_bf16_f32 v101, v102, v103\n"
      "\tds_write_b64 v253, v[100:101] offset:8192\n"
      "\tv_pk_mul_f32 v[104:105], v[104:105], v[12:13] op_sel_hi:[1,0]\n"
      "\tv_pk_mul_f32 v[106:107], v[106:107], v[12:13] op_sel_hi:[1,0]\n"
      "\tv_cvt_pk_bf16_f32 v104, v104, v105\n"
      "\tv_cvt_pk_bf16_f32 v105, v106, v107\n"
      "\tds_write_b64 v254, v[104:105] offset:8192\n"
      "\tv_pk_mul_f32 v[108:109], v[108:109], v[12:13] op_sel_hi:[1,0]\n"
      "\tv_pk_mul_f32 v[110:111], v[110:111], v[12:13] op_sel_hi:[1,0]\n"
      "\tv_cvt_pk_bf16_f32 v108, v108, v109\n"
      "\tv_cvt_pk_bf16_f32 v109, v110, v111\n"
      "\tds_write_b64 v255, v[108:109] offset:8192\n"
      "\tds_write_b32 v176, v179 offset:256\n"
      "\ts_branch Lend_%=\n"
      "Lself_%=:\n"
      "\tv_lshrrev_b32 v5, 3, v2\n"
      "\tv_and_b32 v13, 7, v2\n"
      "\tv_lshrrev_b32 v8, 4, v2\n"
      "\tv_xor_b32 v8, v13, v8\n"
      "\tv_lshl_add_u32 v8, v5, 3, v8\n"
      "\tv_mul_u32_u24 v5, 6144, v5\n"
      "\tv_lshl_add_u32 v8, v8, 3, s57\n"
      "\tv_lshl_add_u32 v5, v13, 4, v5\n"
      "\tv_xor_b32 v6, 32, v8\n"
      "\tds_read2st64_b64 v[192:195], v8 offset0:0 offset1:4\n"
      "\tds_read2st64_b64 v[196:199], v6 offset0:1 offset1:5\n"
      "\tds_read2st64_b64 v[200:203], v8 offset0:2 offset1:6\n"
      "\tds_read2st64_b64 v[204:207], v6 offset0:3 offset1:7\n"
      "\tv_pk_mul_f32 v[48:49], v[48:49], v[12:13] op_sel_hi:[1,0]\n"
      "\tv_pk_mul_f32 v[50:51], v[50:51], v[12:13] op_sel_hi:[1,0]\n"
      "\tv_cvt_pk_bf16_f32 v48, v48, v49\n"
      "\tv_cvt_pk_bf16_f32 v49, v50, v51\n"
      "\tds_write_b64 v248, v[48:49] offset:4096\n"
      "\tv_pk_mul_f32 v[52:53], v[52:53], v[12:13] op_sel_hi:[1,0]\n"
      "\tv_pk_mul_f32 v[54:55], v[54:55], v[12:13] op_sel_hi:[1,0]\n"
      "\tv_cvt_pk_bf16_f32 v52, v52, v53\n"
      "\tv_cvt_pk_bf16_f32 v53, v54, v55\n"
      "\tds_write_b64 v249, v[52:53] offset:4096\n"
      "\tv_pk_mul_f32 v[56:57], v[56:57], v[12:13] op_sel_hi:[1,0]\n"
      "\tv_pk_mul_f32 v[58:59], v[58:59], v[12:13] op_sel_hi:[1,0]\n"
      "\tv_cvt_pk_bf16_f32 v56, v56, v57\n"
      "\tv_cvt_pk_bf16_f32 v57, v58, v59\n"
      "\tds_write_b64 v250, v[56:57] offset:4096\n"
      "\ts_mov_b32 s97, 0\n"
      "\ts_waitcnt lgkmcnt(6)\n"
      "\tbuffer_store_dwordx4 v[192:195], v5, %[osrd], s97 offen offset:0 sc1\n"
      "\tv_pk_mul_f32 v[60:61], v[60:61], v[12:13] op_sel_hi:[1,0]\n"
      "\tv_pk_mul_f32 v[62:63], v[62:63], v[12:13] op_sel_hi:[1,0]\n"
      "\tv_cvt_pk_bf16_f32 v60, v60, v61\n"
      "\tv_cvt_pk_bf16_f32 v61, v62, v63\n"
      "\tds_write_b64 v251, v[60:61] offset:4096\n"
      "\ts_mov_b32 s97, 49152\n"
      "\ts_waitcnt lgkmcnt(6)\n"
      "\tbuffer_store_dwordx4 v[196:199], v5, %[osrd], s97 offen offset:0 sc1\n"
      "\tv_pk_mul_f32 v[64:65], v[64:65], v[12:13] op_sel_hi:[1,0]\n"
      "\tv_pk_mul_f32 v[66:67], v[66:67], v[12:13] op_sel_hi:[1,0]\n"
      "\tv_cvt_pk_bf16_f32 v64, v64, v65\n"
      "\tv_cvt_pk_bf16_f32 v65, v66, v67\n"
      "\tds_write_b64 v252, v[64:65] offset:4096\n"
      "\ts_mov_b32 s97, 98304\n"
      "\ts_waitcnt lgkmcnt(6)\n"
      "\tbuffer_store_dwordx4 v[200:203], v5, %[osrd], s97 offen offset:0 sc1\n"
      "\tv_pk_mul_f32 v[68:69], v[68:69], v[12:13] op_sel_hi:[1,0]\n"
      "\tv_pk_mul_f32 v[70:71], v[70:71], v[12:13] op_sel_hi:[1,0]\n"
      "\tv_cvt_pk_bf16_f32 v68, v68, v69\n"
      "\tv_cvt_pk_bf16_f32 v69, v70, v71\n"
      "\tds_write_b64 v253, v[68:69] offset:4096\n"
      "\ts_mov_b32 s97, 147456\n"
      "\ts_waitcnt lgkmcnt(6)\n"
      "\tbuffer_store_dwordx4 v[204:207], v5, %[osrd], s97 offen offset:0 sc1\n"
      "\tv_pk_mul_f32 v[72:73], v[72:73], v[12:13] op_sel_hi:[1,0]\n"
      "\tv_pk_mul_f32 v[74:75], v[74:75], v[12:13] op_sel_hi:[1,0]\n"
      "\tv_cvt_pk_bf16_f32 v72, v72, v73\n"
      "\tv_cvt_pk_bf16_f32 v73, v74, v75\n"
      "\tds_write_b64 v254, v[72:73] offset:4096\n"
      "\tv_pk_mul_f32 v[76:77], v[76:77], v[12:13] op_sel_hi:[1,0]\n"
      "\tv_pk_mul_f32 v[78:79], v[78:79], v[12:13] op_sel_hi:[1,0]\n"
      "\tv_cvt_pk_bf16_f32 v76, v76, v77\n"
      "\tv_cvt_pk_bf16_f32 v77, v78, v79\n"
      "\tds_write_b64 v255, v[76:77] offset:4096\n"
      "\tds_read2st64_b64 v[208:211], v8 offset0:8 offset1:12\n"
      "\tds_read2st64_b64 v[212:215], v6 offset0:9 offset1:13\n"
      "\tds_read2st64_b64 v[216:219], v8 offset0:10 offset1:14\n"
      "\tds_read2st64_b64 v[220:223], v6 offset0:11 offset1:15\n"
      "\tv_pk_mul_f32 v[80:81], v[80:81], v[12:13] op_sel_hi:[1,0]\n"
      "\tv_pk_mul_f32 v[82:83], v[82:83], v[12:13] op_sel_hi:[1,0]\n"
      "\tv_cvt_pk_bf16_f32 v80, v80, v81\n"
      "\tv_cvt_pk_bf16_f32 v81, v82, v83\n"
      "\tds_write_b64 v248, v[80:81] offset:8192\n"
      "\tv_pk_mul_f32 v[84:85], v[84:85], v[12:13] op_sel_hi:[1,0]\n"
      "\tv_pk_mul_f32 v[86:87], v[86:87], v[12:13] op_sel_hi:[1,0]\n"
      "\tv_cvt_pk_bf16_f32 v84, v84, v85\n"
      "\tv_cvt_pk_bf16_f32 v85, v86, v87\n"
      "\tds_write_b64 v249, v[84:85] offset:8192\n"
      "\tv_pk_mul_f32 v[88:89], v[88:89], v[12:13] op_sel_hi:[1,0]\n"
      "\tv_pk_mul_f32 v[90:91], v[90:91], v[12:13] op_sel_hi:[1,0]\n"
      "\tv_cvt_pk_bf16_f32 v88, v88, v89\n"
      "\tv_cvt_pk_bf16_f32 v89, v90, v91\n"
      "\tds_write_b64 v250, v[88:89] offset:8192\n"
      "\ts_mov_b32 s97, 0\n"
      "\ts_waitcnt lgkmcnt(6)\n"
      "\tbuffer_store_dwordx4 v[208:211], v5, %[osrd], s97 offen offset:128 sc1\n"
      "\tv_pk_mul_f32 v[92:93], v[92:93], v[12:13] op_sel_hi:[1,0]\n"
      "\tv_pk_mul_f32 v[94:95], v[94:95], v[12:13] op_sel_hi:[1,0]\n"
      "\tv_cvt_pk_bf16_f32 v92, v92, v93\n"
      "\tv_cvt_pk_bf16_f32 v93, v94, v95\n"
      "\tds_write_b64 v251, v[92:93] offset:8192\n"
      "\ts_mov_b32 s97, 49152\n"
      "\ts_waitcnt lgkmcnt(6)\n"
      "\tbuffer_store_dwordx4 v[212:215], v5, %[osrd], s97 offen offset:128 sc1\n"
      "\tv_pk_mul_f32 v[96:97], v[96:97], v[12:13] op_sel_hi:[1,0]\n"
      "\tv_pk_mul_f32 v[98:99], v[98:99], v[12:13] op_sel_hi:[1,0]\n"
      "\tv_cvt_pk_bf16_f32 v96, v96, v97\n"
      "\tv_cvt_pk_bf16_f32 v97, v98, v99\n"
      "\tds_write_b64 v252, v[96:97] offset:8192\n"
      "\ts_mov_b32 s97, 98304\n"
      "\ts_waitcnt lgkmcnt(6)\n"
      "\tbuffer_store_dwordx4 v[216:219], v5, %[osrd], s97 offen offset:128 sc1\n"
      "\tv_pk_mul_f32 v[100:101], v[100:101], v[12:13] op_sel_hi:[1,0]\n"
      "\tv_pk_mul_f32 v[102:103], v[102:103], v[12:13] op_sel_hi:[1,0]\n"
      "\tv_cvt_pk_bf16_f32 v100, v100, v101\n"
      "\tv_cvt_pk_bf16_f32 v101, v102, v103\n"
      "\tds_write_b64 v253, v[100:101] offset:8192\n"
      "\ts_mov_b32 s97, 147456\n"
      "\ts_waitcnt lgkmcnt(6)\n"
      "\tbuffer_store_dwordx4 v[220:223], v5, %[osrd], s97 offen offset:128 sc1\n"
      "\tv_pk_mul_f32 v[104:105], v[104:105], v[12:13] op_sel_hi:[1,0]\n"
      "\tv_pk_mul_f32 v[106:107], v[106:107], v[12:13] op_sel_hi:[1,0]\n"
      "\tv_cvt_pk_bf16_f32 v104, v104, v105\n"
      "\tv_cvt_pk_bf16_f32 v105, v106, v107\n"
      "\tds_write_b64 v254, v[104:105] offset:8192\n"
      "\tv_pk_mul_f32 v[108:109], v[108:109], v[12:13] op_sel_hi:[1,0]\n"
      "\tv_pk_mul_f32 v[110:111], v[110:111], v[12:13] op_sel_hi:[1,0]\n"
      "\tv_cvt_pk_bf16_f32 v108, v108, v109\n"
      "\tv_cvt_pk_bf16_f32 v109, v110, v111\n"
      "\tds_write_b64 v255, v[108:109] offset:8192\n"
      "\tds_read2st64_b64 v[224:227], v8 offset0:16 offset1:20\n"
      "\tds_read2st64_b64 v[228:231], v6 offset0:17 offset1:21\n"
      "\tds_read2st64_b64 v[232:235], v8 offset0:18 offset1:22\n"
      "\tds_read2st64_b64 v[236:239], v6 offset0:19 offset1:23\n"
      "\ts_mov_b32 s97, 0\n"
      "\ts_waitcnt lgkmcnt(3)\n"
      "\tbuffer_store_dwordx4 v[224:227], v5, %[osrd], s97 offen offset:256 sc1\n"
      "\ts_mov_b32 s97, 49152\n"
      "\ts_waitcnt lgkmcnt(2)\n"
      "\tbuffer_store_dwordx4 v[228:231], v5, %[osrd], s97 offen offset:256 sc1\n"
      "\ts_mov_b32 s97, 98304\n"
      "\ts_waitcnt lgkmcnt(1)\n"
      "\tbuffer_store_dwordx4 v[232:235], v5, %[osrd], s97 offen offset:256 sc1\n"
      "\ts_mov_b32 s97, 147456\n"
      "\ts_waitcnt lgkmcnt(0)\n"
      "\tbuffer_store_dwordx4 v[236:239], v5, %[osrd], s97 offen offset:256 sc1\n"
      "\ts_branch Lend_%=\n"
      "Lhelp_%=:\n"
      "\ts_cmp_eq_u32 s59, 3\n"
      "\ts_cbranch_scc1 Lend_%=\n"
      "\tv_mbcnt_lo_u32_b32 v2, -1, 0\n"
      "\tv_mbcnt_hi_u32_b32 v2, -1, v2\n"
      "\tv_lshrrev_b32 v5, 3, v2\n"
      "\tv_and_b32 v13, 7, v2\n"
      "\tv_lshrrev_b32 v8, 4, v2\n"
      "\tv_xor_b32 v8, v13, v8\n"
      "\tv_lshl_add_u32 v8, v5, 3, v8\n"
      "\tv_mul_u32_u24 v5, 6144, v5\n"
      "\tv_lshl_add_u32 v8, v8, 3, s58\n"
      "\tv_lshl_add_u32 v5, v13, 4, v5\n"
      "\tv_xor_b32 v6, 32, v8\n"
      "\tv_mov_b32 v178, s99\n"
      "Lhp0_%=:\n"
      "\tds_read_b32 v179, v178 offset:256\n"
      "\ts_waitcnt lgkmcnt(0)\n"
      "\tv_readfirstlane_b32 s97, v179\n"
      "\ts_nop 3\n"
      "\ts_cmp_gt_u32 s97, 0\n"
      "\ts_cbranch_scc1 Lhd0_%=\n"
      "\ts_sleep 1\n"
      "\ts_branch Lhp0_%=\n"
      "Lhd0_%=:\n"
      "\tds_read2st64_b64 v[192:195], v8 offset0:0 offset1:4\n"
      "\tds_read2st64_b64 v[196:199], v6 offset0:1 offset1:5\n"
      "\tds_read2st64_b64 v[200:203], v8 offset0:2 offset1:6\n"
      "\tds_read2st64_b64 v[204:207], v6 offset0:3 offset1:7\n"
      "\ts_mov_b32 s97, 0\n"
      "\ts_waitcnt lgkmcnt(3)\n"
      "\tbuffer_store_dwordx4 v[192:195], v5, %[osrd], s97 offen offset:0 sc1\n"
      "\ts_mov_b32 s97, 49152\n"
      "\ts_waitcnt lgkmcnt(2)\n"
      "\tbuffer_store_dwordx4 v[196:199], v5, %[osrd], s97 offen offset:0 sc1\n"
      "\ts_mov_b32 s97, 98304\n"
      "\ts_waitcnt lgkmcnt(1)\n"
      "\tbuffer_store_dwordx4 v[200:203], v5, %[osrd], s97 offen offset:0 sc1\n"
      "\ts_mov_b32 s97, 147456\n"
      "\ts_waitcnt lgkmcnt(0)\n"
      "\tbuffer_store_dwordx4 v[204:207], v5, %[osrd], s97 offen offset:0 sc1\n"
      "Lhp1_%=:\n"
      "\tds_read_b32 v179, v178 offset:256\n"
      "\ts_waitcnt lgkmcnt(0)\n"
      "\tv_readfirstlane_b32 s97, v179\n"
      "\ts_nop 3\n"
      "\ts_cmp_gt_u32 s97, 1\n"
      "\ts_cbranch_scc1 Lhd1_%=\n"
      "\ts_sleep 1\n"
      "\ts_branch Lhp1_%=\n"
      "Lhd1_%=:\n"
      "\tds_read2st64_b64 v[208:211], v8 offset0:8 offset1:12\n"
      "\tds_read2st64_b64 v[212:215], v6 offset0:9 offset1:13\n"
      "\tds_read2st64_b64 v[216:219], v8 offset0:10 offset1:14\n"
      "\tds_read2st64_b64 v[220:223], v6 offset0:11 offset1:15\n"
      "\ts_mov_b32 s97, 0\n"
      "\ts_waitcnt lgkmcnt(3)\n"
      "\tbuffer_store_dwordx4 v[208:211], v5, %[osrd], s97 offen offset:128 sc1\n"
      "\ts_mov_b32 s97, 49152\n"
      "\ts_waitcnt lgkmcnt(2)\n"
      "\tbuffer_store_dwordx4 v[212:215], v5, %[osrd], s97 offen offset:128 sc1\n"
      "\ts_mov_b32 s97, 98304\n"
      "\ts_waitcnt lgkmcnt(1)\n"
      "\tbuffer_store_dwordx4 v[216:219], v5, %[osrd], s97 offen offset:128 sc1\n"
      "\ts_mov_b32 s97, 147456\n"
      "\ts_waitcnt lgkmcnt(0)\n"
      "\tbuffer_store_dwordx4 v[220:223], v5, %[osrd], s97 offen offset:128 sc1\n"
      "Lhp2_%=:\n"
      "\tds_read_b32 v179, v178 offset:256\n"
      "\ts_waitcnt lgkmcnt(0)\n"
      "\tv_readfirstlane_b32 s97, v179\n"
      "\ts_nop 3\n"
      "\ts_cmp_gt_u32 s97, 2\n"
      "\ts_cbranch_scc1 Lhd2_%=\n"
      "\ts_sleep 1\n"
      "\ts_branch Lhp2_%=\n"
      "Lhd2_%=:\n"
      "\tds_read2st64_b64 v[224:227], v8 offset0:16 offset1:20\n"
      "\tds_read2st64_b64 v[228:231], v6 offset0:17 offset1:21\n"
      "\tds_read2st64_b64 v[232:235], v8 offset0:18 offset1:22\n"
      "\tds_read2st64_b64 v[236:239], v6 offset0:19 offset1:23\n"
      "\ts_mov_b32 s97, 0\n"
      "\ts_waitcnt lgkmcnt(3)\n"
      "\tbuffer_store_dwordx4 v[224:227], v5, %[osrd], s97 offen offset:256 sc1\n"
      "\ts_mov_b32 s97, 49152\n"
      "\ts_waitcnt lgkmcnt(2)\n"
      "\tbuffer_store_dwordx4 v[228:231], v5, %[osrd], s97 offen offset:256 sc1\n"
      "\ts_mov_b32 s97, 98304\n"
      "\ts_waitcnt lgkmcnt(1)\n"
      "\tbuffer_store_dwordx4 v[232:235], v5, %[osrd], s97 offen offset:256 sc1\n"
      "\ts_mov_b32 s97, 147456\n"
      "\ts_waitcnt lgkmcnt(0)\n"
      "\tbuffer_store_dwordx4 v[236:239], v5, %[osrd], s97 offen offset:256 sc1\n"
      "\ts_branch Lend_%=\n"
      "Lpair_%=:\n"
      "\tv_mfma_f32_32x32x16_bf16 v[48:63], v[200:203], v[112:115], v[48:63]\n"
      "\tv_lshlrev_b32 v3, 4, v2\n"
      "\tv_and_b32 v15, 31, v2\n"
      "\tv_mfma_f32_32x32x16_bf16 v[48:63], v[224:227], v[116:119], v[48:63]\n"
      "\tv_add_u32 v4, s58, v3\n"
      "\tv_add_u32 v3, s57, v3\n"
      "\tv_mfma_f32_32x32x16_bf16 v[64:79], v[204:207], v[112:115], v[64:79]\n"
      "\tv_lshlrev_b32 v15, 3, v15\n"
      "\tv_add_u32 v9, s99, v15\n"
      "\tv_mfma_f32_32x32x16_bf16 v[64:79], v[228:231], v[116:119], v[64:79]\n"
      "\tv_add_u32 v15, s98, v15\n"
      "\tds_write_b64 v15, v[10:11]\n"
      "\tv_mfma_f32_32x32x16_bf16 v[80:95], v[208:211], v[112:115], v[80:95]\n"
      "\tv_lshrrev_b32 v5, 3, v2\n"
      "\tv_and_b32 v13, 7, v2\n"
      "\tv_mfma_f32_32x32x16_bf16 v[80:95], v[232:235], v[116:119], v[80:95]\n"
      "\tv_lshrrev_b32 v8, 4, v2\n"
      "\tv_xor_b32 v8, v13, v8\n"
      "\tv_mfma_f32_32x32x16_bf16 v[96:111], v[212:215], v[112:115], v[96:111]\n"
      "\tv_lshl_add_u32 v8, v5, 3, v8\n"
      "\tv_mul_u32_u24 v5, 6144, v5\n"
      "\tv_mfma_f32_32x32x16_bf16 v[96:111], v[236:239], v[116:119], v[96:111]\n"
      "\tv_lshl_add_u32 v8, v8, 3, s58\n"
      "\tv_lshl_add_u32 v5, v13, 4, v5\n"
      "\tv_xor_b32 v6, 32, v8\n"
      "\ts_nop 7\n"
      "\ts_cmp_eq_u32 s59, 1\n"
      "\ts_cbranch_scc1 Llate_%=\n"
      "\tds_write_b128 v3, v[16:19] offset:0\n"
      "\tds_write_b128 v3, v[20:23] offset:1024\n"
      "\tds_write_b128 v3, v[24:27] offset:2048\n"
      "\tds_write_b128 v3, v[28:31] offset:3072\n"
      "\tds_write_b128 v3, v[32:35] offset:4096\n"
      "\tds_write_b128 v3, v[36:39] offset:5120\n"
      "\tds_write_b128 v3, v[40:43] offset:6144\n"
      "\tds_write_b128 v3, v[44:47] offset:7168\n"
      "\tds_write_b128 v3, v[48:51] offset:8192\n"
      "\tds_write_b128 v3, v[52:55] offset:9216\n"
      "\tds_write_b128 v3, v[56:59] offset:10240\n"
      "\tds_write_b128 v3, v[60:63] offset:11264\n"
      "\tds_write_b128 v4, v[64:67] offset:0\n"
      "\tds_write_b128 v4, v[68:71] offset:1024\n"
      "\tds_write_b128 v4, v[72:75] offset:2048\n"
      "\tds_write_b128 v4, v[76:79] offset:3072\n"
      "\ts_branch Lsent_%=\n"
      "Llate_%=:\n"
      "\tds_write_b128 v3, v[80:83] offset:4096\n"
      "\tds_write_b128 v3, v[84:87] offset:5120\n"
      "\tds_write_b128 v3, v[88:91] offset:6144\n"
      "\tds_write_b128 v3, v[92:95] offset:7168\n"
      "\tds_write_b128 v3, v[96:99] offset:8192\n"
      "\tds_write_b128 v3, v[100:103] offset:9216\n"
      "\tds_write_b128 v3, v[104:107] offset:10240\n"
      "\tds_write_b128 v3, v[108:111] offset:11264\n"
      "Lsent_%=:\n"
      "\tv_mov_b32 v176, s98\n"
      "\tv_mov_b32 v177, 1\n"
      "\tds_write_b32 v176, v177 offset:256\n"
      "\tv_mov_b32 v178, s99\n"
      "Lpo_%=:\n"
      "\tds_read_b32 v179, v178 offset:256\n"
      "\ts_waitcnt lgkmcnt(0)\n"
      "\tv_readfirstlane_b32 s97, v179\n"
      "\ts_nop 3\n"
      "\ts_cmp_lg_u32 s97, 0\n"
      "\ts_cbranch_scc1 Lpd_%=\n"
      "\ts_sleep 2\n"
      "\ts_branch Lpo_%=\n"
      "Lpd_%=:\n"
      "\tds_read_b64 v[180:181], v9\n"
      "\tds_read_b128 v[112:115], v4 offset:0\n"
      "\tds_read_b128 v[116:119], v4 offset:1024\n"
      "\tds_read_b128 v[120:123], v4 offset:2048\n"
      "\tds_read_b128 v[124:127], v4 offset:3072\n"
      "\tds_read_b128 v[128:131], v4 offset:4096\n"
      "\tds_read_b128 v[132:135], v4 offset:5120\n"
      "\tds_read_b128 v[136:139], v4 offset:6144\n"
      "\tds_read_b128 v[140:143], v4 offset:7168\n"
      "\tds_read_b128 v[144:147], v4 offset:8192\n"
      "\tds_read_b128 v[148:151], v4 offset:9216\n"
      "\tds_read_b128 v[152:155], v4 offset:10240\n"
      "\tds_read_b128 v[156:159], v4 offset:11264\n"
      "\ts_waitcnt lgkmcnt(12)\n"
      "\tv_min_f32 v182, v10, v180\n"
      "\tv_sub_f32 v12, v182, v10\n"
      "\tv_sub_f32 v14, v182, v180\n"
      "\tv_exp_f32 v12, v12\n"
      "\tv_exp_f32 v14, v14\n"
      "\ts_nop 0\n"
      "\tv_mul_f32 v182, v11, v12\n"
      "\tv_fmac_f32 v182, v181, v14\n"
      "\tv_rcp_f32 v182, v182\n"
      "\ts_nop 0\n"
      "\tv_mul_f32 v12, v12, v182\n"
      "\tv_mul_f32 v14, v14, v182\n"
      "\ts_cmp_eq_u32 s59, 1\n"
      "\ts_waitcnt lgkmcnt(0)\n"
      "\ts_cbranch_scc1 Lfl_%=\n"
      "\tv_pk_mul_f32 v[80:81], v[80:81], v[12:13] op_sel_hi:[1,0]\n"
      "\tv_pk_mul_f32 v[82:83], v[82:83], v[12:13] op_sel_hi:[1,0]\n"
      "\tv_pk_fma_f32 v[80:81], v[128:129], v[14:15], v[80:81] op_sel_hi:[1,0,1]\n"
      "\tv_pk_fma_f32 v[82:83], v[130:131], v[14:15], v[82:83] op_sel_hi:[1,0,1]\n"
      "\tv_cvt_pk_bf16_f32 v80, v80, v81\n"
      "\tv_cvt_pk_bf16_f32 v81, v82, v83\n"
      "\tds_write_b64 v248, v[80:81] offset:8192\n"
      "\tv_pk_mul_f32 v[84:85], v[84:85], v[12:13] op_sel_hi:[1,0]\n"
      "\tv_pk_mul_f32 v[86:87], v[86:87], v[12:13] op_sel_hi:[1,0]\n"
      "\tv_pk_fma_f32 v[84:85], v[132:133], v[14:15], v[84:85] op_sel_hi:[1,0,1]\n"
      "\tv_pk_fma_f32 v[86:87], v[134:135], v[14:15], v[86:87] op_sel_hi:[1,0,1]\n"
      "\tv_cvt_pk_bf16_f32 v84, v84, v85\n"
      "\tv_cvt_pk_bf16_f32 v85, v86, v87\n"
      "\tds_write_b64 v249, v[84:85] offset:8192\n"
      "\tv_pk_mul_f32 v[88:89], v[88:89], v[12:13] op_sel_hi:[1,0]\n"
      "\tv_pk_mul_f32 v[90:91], v[90:91], v[12:13] op_sel_hi:[1,0]\n"
      "\tv_pk_fma_f32 v[88:89], v[136:137], v[14:15], v[88:89] op_sel_hi:[1,0,1]\n"
      "\tv_pk_fma_f32 v[90:91], v[138:139], v[14:15], v[90:91] op_sel_hi:[1,0,1]\n"
      "\tv_cvt_pk_bf16_f32 v88, v88, v89\n"
      "\tv_cvt_pk_bf16_f32 v89, v90, v91\n"
      "\tds_write_b64 v250, v[88:89] offset:8192\n"
      "\tv_pk_mul_f32 v[92:93], v[92:93], v[12:13] op_sel_hi:[1,0]\n"
      "\tv_pk_mul_f32 v[94:95], v[94:95], v[12:13] op_sel_hi:[1,0]\n"
      "\tv_pk_fma_f32 v[92:93], v[140:141], v[14:15], v[92:93] op_sel_hi:[1,0,1]\n"
      "\tv_pk_fma_f32 v[94:95], v[142:143], v[14:15], v[94:95] op_sel_hi:[1,0,1]\n"
      "\tv_cvt_pk_bf16_f32 v92, v92, v93\n"
      "\tv_cvt_pk_bf16_f32 v93, v94, v95\n"
      "\tds_write_b64 v251, v[92:93] offset:8192\n"
      "\tv_pk_mul_f32 v[96:97], v[96:97], v[12:13] op_sel_hi:[1,0]\n"
      "\tv_pk_mul_f32 v[98:99], v[98:99], v[12:13] op_sel_hi:[1,0]\n"
      "\tv_pk_fma_f32 v[96:97], v[144:145], v[14:15], v[96:97] op_sel_hi:[1,0,1]\n"
      "\tv_pk_fma_f32 v[98:99], v[146:147], v[14:15], v[98:99] op_sel_hi:[1,0,1]\n"
      "\tv_cvt_pk_bf16_f32 v96, v96, v97\n"
      "\tv_cvt_pk_bf16_f32 v97, v98, v99\n"
      "\tds_write_b64 v252, v[96:97] offset:8192\n"
      "\tv_pk_mul_f32 v[100:101], v[100:101], v[12:13] op_sel_hi:[1,0]\n"
      "\tv_pk_mul_f32 v[102:103], v[102:103], v[12:13] op_sel_hi:[1,0]\n"
      "\tv_pk_fma_f32 v[100:101], v[148:149], v[14:15], v[100:101] op_sel_hi:[1,0,1]\n"
      "\tv_pk_fma_f32 v[102:103], v[150:151], v[14:15], v[102:103] op_sel_hi:[1,0,1]\n"
      "\tv_cvt_pk_bf16_f32 v100, v100, v101\n"
      "\tv_cvt_pk_bf16_f32 v101, v102, v103\n"
      "\tds_write_b64 v253, v[100:101] offset:8192\n"
      "\tv_pk_mul_f32 v[104:105], v[104:105], v[12:13] op_sel_hi:[1,0]\n"
      "\tv_pk_mul_f32 v[106:107], v[106:107], v[12:13] op_sel_hi:[1,0]\n"
      "\tv_pk_fma_f32 v[104:105], v[152:153], v[14:15], v[104:105] op_sel_hi:[1,0,1]\n"
      "\tv_pk_fma_f32 v[106:107], v[154:155], v[14:15], v[106:107] op_sel_hi:[1,0,1]\n"
      "\tv_cvt_pk_bf16_f32 v104, v104, v105\n"
      "\tv_cvt_pk_bf16_f32 v105, v106, v107\n"
      "\tds_write_b64 v254, v[104:105] offset:8192\n"
      "\tv_pk_mul_f32 v[108:109], v[108:109], v[12:13] op_sel_hi:[1,0]\n"
      "\tv_pk_mul_f32 v[110:111], v[110:111], v[12:13] op_sel_hi:[1,0]\n"
      "\tv_pk_fma_f32 v[108:109], v[156:157], v[14:15], v[108:109] op_sel_hi:[1,0,1]\n"
      "\tv_pk_fma_f32 v[110:111], v[158:159], v[14:15], v[110:111] op_sel_hi:[1,0,1]\n"
      "\tv_cvt_pk_bf16_f32 v108, v108, v109\n"
      "\tv_cvt_pk_bf16_f32 v109, v110, v111\n"
      "\tds_write_b64 v255, v[108:109] offset:8192\n"
      "\tds_read2st64_b64 v[224:227], v8 offset0:16 offset1:20\n"
      "\tds_read2st64_b64 v[228:231], v6 offset0:17 offset1:21\n"
      "\tds_read2st64_b64 v[232:235], v8 offset0:18 offset1:22\n"
      "\tds_read2st64_b64 v[236:239], v6 offset0:19 offset1:23\n"
      "\ts_mov_b32 s97, 0\n"
      "\ts_waitcnt lgkmcnt(3)\n"
      "\tbuffer_store_dwordx4 v[224:227], v5, %[osrd], s97 offen offset:256 sc1\n"
      "\ts_mov_b32 s97, 49152\n"
      "\ts_waitcnt lgkmcnt(2)\n"
      "\tbuffer_store_dwordx4 v[228:231], v5, %[osrd], s97 offen offset:256 sc1\n"
      "\ts_mov_b32 s97, 98304\n"
      "\ts_waitcnt lgkmcnt(1)\n"
      "\tbuffer_store_dwordx4 v[232:235], v5, %[osrd], s97 offen offset:256 sc1\n"
      "\ts_mov_b32 s97, 147456\n"
      "\ts_waitcnt lgkmcnt(0)\n"
      "\tbuffer_store_dwordx4 v[236:239], v5, %[osrd], s97 offen offset:256 sc1\n"
      "\ts_branch Lend_%=\n"
      "Lfl_%=:\n"
      "\tds_read_b128 v[160:163], v3 offset:0\n"
      "\tds_read_b128 v[164:167], v3 offset:1024\n"
      "\tds_read_b128 v[168:171], v3 offset:2048\n"
      "\tds_read_b128 v[172:175], v3 offset:3072\n"
      "\tv_pk_mul_f32 v[16:17], v[16:17], v[12:13] op_sel_hi:[1,0]\n"
      "\tv_pk_mul_f32 v[18:19], v[18:19], v[12:13] op_sel_hi:[1,0]\n"
      "\tv_pk_fma_f32 v[16:17], v[112:113], v[14:15], v[16:17] op_sel_hi:[1,0,1]\n"
      "\tv_pk_fma_f32 v[18:19], v[114:115], v[14:15], v[18:19] op_sel_hi:[1,0,1]\n"
      "\tv_cvt_pk_bf16_f32 v16, v16, v17\n"
      "\tv_cvt_pk_bf16_f32 v17, v18, v19\n"
      "\tds_write_b64 v248, v[16:17] offset:0\n"
      "\tv_pk_mul_f32 v[20:21], v[20:21], v[12:13] op_sel_hi:[1,0]\n"
      "\tv_pk_mul_f32 v[22:23], v[22:23], v[12:13] op_sel_hi:[1,0]\n"
      "\tv_pk_fma_f32 v[20:21], v[116:117], v[14:15], v[20:21] op_sel_hi:[1,0,1]\n"
      "\tv_pk_fma_f32 v[22:23], v[118:119], v[14:15], v[22:23] op_sel_hi:[1,0,1]\n"
      "\tv_cvt_pk_bf16_f32 v20, v20, v21\n"
      "\tv_cvt_pk_bf16_f32 v21, v22, v23\n"
      "\tds_write_b64 v249, v[20:21] offset:0\n"
      "\tv_pk_mul_f32 v[24:25], v[24:25], v[12:13] op_sel_hi:[1,0]\n"
      "\tv_pk_mul_f32 v[26:27], v[26:27], v[12:13] op_sel_hi:[1,0]\n"
      "\tv_pk_fma_f32 v[24:25], v[120:121], v[14:15], v[24:25] op_sel_hi:[1,0,1]\n"
      "\tv_pk_fma_f32 v[26:27], v[122:123], v[14:15], v[26:27] op_sel_hi:[1,0,1]\n"
      "\tv_cvt_pk_bf16_f32 v24, v24, v25\n"
      "\tv_cvt_pk_bf16_f32 v25, v26, v27\n"
      "\tds_write_b64 v250, v[24:25] offset:0\n"
      "\tv_pk_mul_f32 v[28:29], v[28:29], v[12:13] op_sel_hi:[1,0]\n"
      "\tv_pk_mul_f32 v[30:31], v[30:31], v[12:13] op_sel_hi:[1,0]\n"
      "\tv_pk_fma_f32 v[28:29], v[124:125], v[14:15], v[28:29] op_sel_hi:[1,0,1]\n"
      "\tv_pk_fma_f32 v[30:31], v[126:127], v[14:15], v[30:31] op_sel_hi:[1,0,1]\n"
      "\tv_cvt_pk_bf16_f32 v28, v28, v29\n"
      "\tv_cvt_pk_bf16_f32 v29, v30, v31\n"
      "\tds_write_b64 v251, v[28:29] offset:0\n"
      "\tv_pk_mul_f32 v[32:33], v[32:33], v[12:13] op_sel_hi:[1,0]\n"
      "\tv_pk_mul_f32 v[34:35], v[34:35], v[12:13] op_sel_hi:[1,0]\n"
      "\tv_pk_fma_f32 v[32:33], v[128:129], v[14:15], v[32:33] op_sel_hi:[1,0,1]\n"
      "\tv_pk_fma_f32 v[34:35], v[130:131], v[14:15], v[34:35] op_sel_hi:[1,0,1]\n"
      "\tv_cvt_pk_bf16_f32 v32, v32, v33\n"
      "\tv_cvt_pk_bf16_f32 v33, v34, v35\n"
      "\tds_write_b64 v252, v[32:33] offset:0\n"
      "\tv_pk_mul_f32 v[36:37], v[36:37], v[12:13] op_sel_hi:[1,0]\n"
      "\tv_pk_mul_f32 v[38:39], v[38:39], v[12:13] op_sel_hi:[1,0]\n"
      "\tv_pk_fma_f32 v[36:37], v[132:133], v[14:15], v[36:37] op_sel_hi:[1,0,1]\n"
      "\tv_pk_fma_f32 v[38:39], v[134:135], v[14:15], v[38:39] op_sel_hi:[1,0,1]\n"
      "\tv_cvt_pk_bf16_f32 v36, v36, v37\n"
      "\tv_cvt_pk_bf16_f32 v37, v38, v39\n"
      "\tds_write_b64 v253, v[36:37] offset:0\n"
      "\tv_pk_mul_f32 v[40:41], v[40:41], v[12:13] op_sel_hi:[1,0]\n"
      "\tv_pk_mul_f32 v[42:43], v[42:43], v[12:13] op_sel_hi:[1,0]\n"
      "\tv_pk_fma_f32 v[40:41], v[136:137], v[14:15], v[40:41] op_sel_hi:[1,0,1]\n"
      "\tv_pk_fma_f32 v[42:43], v[138:139], v[14:15], v[42:43] op_sel_hi:[1,0,1]\n"
      "\tv_cvt_pk_bf16_f32 v40, v40, v41\n"
      "\tv_cvt_pk_bf16_f32 v41, v42, v43\n"
      "\tds_write_b64 v254, v[40:41] offset:0\n"
      "\tv_pk_mul_f32 v[44:45], v[44:45], v[12:13] op_sel_hi:[1,0]\n"
      "\tv_pk_mul_f32 v[46:47], v[46:47], v[12:13] op_sel_hi:[1,0]\n"
      "\tv_pk_fma_f32 v[44:45], v[140:141], v[14:15], v[44:45] op_sel_hi:[1,0,1]\n"
      "\tv_pk_fma_f32 v[46:47], v[142:143], v[14:15], v[46:47] op_sel_hi:[1,0,1]\n"
      "\tv_cvt_pk_bf16_f32 v44, v44, v45\n"
      "\tv_cvt_pk_bf16_f32 v45, v46, v47\n"
      "\tds_write_b64 v255, v[44:45] offset:0\n"
      "\tds_read2st64_b64 v[192:195], v8 offset0:0 offset1:4\n"
      "\tds_read2st64_b64 v[196:199], v6 offset0:1 offset1:5\n"
      "\tds_read2st64_b64 v[200:203], v8 offset0:2 offset1:6\n"
      "\tds_read2st64_b64 v[204:207], v6 offset0:3 offset1:7\n"
      "\tv_pk_mul_f32 v[48:49], v[48:49], v[12:13] op_sel_hi:[1,0]\n"
      "\tv_pk_mul_f32 v[50:51], v[50:51], v[12:13] op_sel_hi:[1,0]\n"
      "\tv_pk_fma_f32 v[48:49], v[144:145], v[14:15], v[48:49] op_sel_hi:[1,0,1]\n"
      "\tv_pk_fma_f32 v[50:51], v[146:147], v[14:15], v[50:51] op_sel_hi:[1,0,1]\n"
      "\tv_cvt_pk_bf16_f32 v48, v48, v49\n"
      "\tv_cvt_pk_bf16_f32 v49, v50, v51\n"
      "\tds_write_b64 v248, v[48:49] offset:4096\n"
      "\tv_pk_mul_f32 v[52:53], v[52:53], v[12:13] op_sel_hi:[1,0]\n"
      "\tv_pk_mul_f32 v[54:55], v[54:55], v[12:13] op_sel_hi:[1,0]\n"
      "\tv_pk_fma_f32 v[52:53], v[148:149], v[14:15], v[52:53] op_sel_hi:[1,0,1]\n"
      "\tv_pk_fma_f32 v[54:55], v[150:151], v[14:15], v[54:55] op_sel_hi:[1,0,1]\n"
      "\tv_cvt_pk_bf16_f32 v52, v52, v53\n"
      "\tv_cvt_pk_bf16_f32 v53, v54, v55\n"
      "\tds_write_b64 v249, v[52:53] offset:4096\n"
      "\tv_pk_mul_f32 v[56:57], v[56:57], v[12:13] op_sel_hi:[1,0]\n"
      "\tv_pk_mul_f32 v[58:59], v[58:59], v[12:13] op_sel_hi:[1,0]\n"
      "\tv_pk_fma_f32 v[56:57], v[152:153], v[14:15], v[56:57] op_sel_hi:[1,0,1]\n"
      "\tv_pk_fma_f32 v[58:59], v[154:155], v[14:15], v[58:59] op_sel_hi:[1,0,1]\n"
      "\tv_cvt_pk_bf16_f32 v56, v56, v57\n"
      "\tv_cvt_pk_bf16_f32 v57, v58, v59\n"
      "\tds_write_b64 v250, v[56:57] offset:4096\n"
      "\ts_mov_b32 s97, 0\n"
      "\ts_waitcnt lgkmcnt(6)\n"
      "\tbuffer_store_dwordx4 v[192:195], v5, %[osrd], s97 offen offset:0 sc1\n"
      "\tv_pk_mul_f32 v[60:61], v[60:61], v[12:13] op_sel_hi:[1,0]\n"
      "\tv_pk_mul_f32 v[62:63], v[62:63], v[12:13] op_sel_hi:[1,0]\n"
      "\tv_pk_fma_f32 v[60:61], v[156:157], v[14:15], v[60:61] op_sel_hi:[1,0,1]\n"
      "\tv_pk_fma_f32 v[62:63], v[158:159], v[14:15], v[62:63] op_sel_hi:[1,0,1]\n"
      "\tv_cvt_pk_bf16_f32 v60, v60, v61\n"
      "\tv_cvt_pk_bf16_f32 v61, v62, v63\n"
      "\tds_write_b64 v251, v[60:61] offset:4096\n"
      "\ts_mov_b32 s97, 49152\n"
      "\ts_waitcnt lgkmcnt(6)\n"
      "\tbuffer_store_dwordx4 v[196:199], v5, %[osrd], s97 offen offset:0 sc1\n"
      "\tv_pk_mul_f32 v[64:65], v[64:65], v[12:13] op_sel_hi:[1,0]\n"
      "\tv_pk_mul_f32 v[66:67], v[66:67], v[12:13] op_sel_hi:[1,0]\n"
      "\tv_pk_fma_f32 v[64:65], v[160:161], v[14:15], v[64:65] op_sel_hi:[1,0,1]\n"
      "\tv_pk_fma_f32 v[66:67], v[162:163], v[14:15], v[66:67] op_sel_hi:[1,0,1]\n"
      "\tv_cvt_pk_bf16_f32 v64, v64, v65\n"
      "\tv_cvt_pk_bf16_f32 v65, v66, v67\n"
      "\tds_write_b64 v252, v[64:65] offset:4096\n"
      "\ts_mov_b32 s97, 98304\n"
      "\ts_waitcnt lgkmcnt(6)\n"
      "\tbuffer_store_dwordx4 v[200:203], v5, %[osrd], s97 offen offset:0 sc1\n"
      "\tv_pk_mul_f32 v[68:69], v[68:69], v[12:13] op_sel_hi:[1,0]\n"
      "\tv_pk_mul_f32 v[70:71], v[70:71], v[12:13] op_sel_hi:[1,0]\n"
      "\tv_pk_fma_f32 v[68:69], v[164:165], v[14:15], v[68:69] op_sel_hi:[1,0,1]\n"
      "\tv_pk_fma_f32 v[70:71], v[166:167], v[14:15], v[70:71] op_sel_hi:[1,0,1]\n"
      "\tv_cvt_pk_bf16_f32 v68, v68, v69\n"
      "\tv_cvt_pk_bf16_f32 v69, v70, v71\n"
      "\tds_write_b64 v253, v[68:69] offset:4096\n"
      "\ts_mov_b32 s97, 147456\n"
      "\ts_waitcnt lgkmcnt(6)\n"
      "\tbuffer_store_dwordx4 v[204:207], v5, %[osrd], s97 offen offset:0 sc1\n"
      "\tv_pk_mul_f32 v[72:73], v[72:73], v[12:13] op_sel_hi:[1,0]\n"
      "\tv_pk_mul_f32 v[74:75], v[74:75], v[12:13] op_sel_hi:[1,0]\n"
      "\tv_pk_fma_f32 v[72:73], v[168:169], v[14:15], v[72:73] op_sel_hi:[1,0,1]\n"
      "\tv_pk_fma_f32 v[74:75], v[170:171], v[14:15], v[74:75] op_sel_hi:[1,0,1]\n"
      "\tv_cvt_pk_bf16_f32 v72, v72, v73\n"
      "\tv_cvt_pk_bf16_f32 v73, v74, v75\n"
      "\tds_write_b64 v254, v[72:73] offset:4096\n"
      "\tv_pk_mul_f32 v[76:77], v[76:77], v[12:13] op_sel_hi:[1,0]\n"
      "\tv_pk_mul_f32 v[78:79], v[78:79], v[12:13] op_sel_hi:[1,0]\n"
      "\tv_pk_fma_f32 v[76:77], v[172:173], v[14:15], v[76:77] op_sel_hi:[1,0,1]\n"
      "\tv_pk_fma_f32 v[78:79], v[174:175], v[14:15], v[78:79] op_sel_hi:[1,0,1]\n"
      "\tv_cvt_pk_bf16_f32 v76, v76, v77\n"
      "\tv_cvt_pk_bf16_f32 v77, v78, v79\n"
      "\tds_write_b64 v255, v[76:77] offset:4096\n"
      "\tds_read2st64_b64 v[208:211], v8 offset0:8 offset1:12\n"
      "\tds_read2st64_b64 v[212:215], v6 offset0:9 offset1:13\n"
      "\tds_read2st64_b64 v[216:219], v8 offset0:10 offset1:14\n"
      "\tds_read2st64_b64 v[220:223], v6 offset0:11 offset1:15\n"
      "\ts_mov_b32 s97, 0\n"
      "\ts_waitcnt lgkmcnt(3)\n"
      "\tbuffer_store_dwordx4 v[208:211], v5, %[osrd], s97 offen offset:128 sc1\n"
      "\ts_mov_b32 s97, 49152\n"
      "\ts_waitcnt lgkmcnt(2)\n"
      "\tbuffer_store_dwordx4 v[212:215], v5, %[osrd], s97 offen offset:128 sc1\n"
      "\ts_mov_b32 s97, 98304\n"
      "\ts_waitcnt lgkmcnt(1)\n"
      "\tbuffer_store_dwordx4 v[216:219], v5, %[osrd], s97 offen offset:128 sc1\n"
      "\ts_mov_b32 s97, 147456\n"
      "\ts_waitcnt lgkmcnt(0)\n"
      "\tbuffer_store_dwordx4 v[220:223], v5, %[osrd], s97 offen offset:128 sc1\n"
      "Lend_%=:\n"
      "\ts_setprio 0\n"
      "\ts_mov_b32 m0, s92\n"
      "\ts_nop 1\n"
      : "+{v[2:9]}"(va), "+{v240}"(pf0), "+{v241}"(pf1), "+{s[72:87]}"(sa), "+{s[88:91]}"(sb), "+{s[56:59]}"(sc)
      : "{s98}"(stat), "{s99}"(pstat), [ksrd] "s"(ksrd), [vsrd] "s"(vsrd), [osrd] "s"(osrd)
      : "memory", "scc", "vcc", "v1", "v10", "v11", "v12", "v13", "v14", "v15", "v16", "v17", "v18", "v19", "v20", "v21", "v22", "v23", "v24", "v25", "v26", "v27", "v28", "v29", "v30", "v31", "v32", "v33", "v34", "v35", "v36", "v37", "v38", "v39", "v40", "v41", "v42", "v43", "v44", "v45", "v46", "v47", "v48", "v49", "v50", "v51", "v52", "v53", "v54", "v55", "v56", "v57", "v58", "v59", "v60", "v61", "v62", "v63", "v64", "v65", "v66", "v67", "v68", "v69", "v70", "v71", "v72", "v73", "v74", "v75", "v76", "v77", "v78", "v79", "v80", "v81", "v82", "v83", "v84", "v85", "v86", "v87", "v88", "v89", "v90", "v91", "v92", "v93", "v94", "v95", "v96", "v97", "v98", "v99", "v100", "v101", "v102", "v103", "v104", "v105", "v106", "v107", "v108", "v109", "v110", "v111", "v112", "v113", "v114", "v115", "v116", "v117", "v118", "v119", "v120", "v121", "v122", "v123", "v124", "v125", "v126", "v127", "v128", "v129", "v130", "v131", "v132", "v133", "v134", "v135", "v136", "v137", "v138", "v139", "v140", "v141", "v142", "v143", "v144", "v145", "v146", "v147", "v148", "v149", "v150", "v151", "v152", "v153", "v154", "v155", "v156", "v157", "v158", "v159", "v160", "v161", "v162", "v163", "v164", "v165", "v166", "v167", "v168", "v169", "v170", "v171", "v172", "v173", "v174", "v175", "v176", "v177", "v178", "v179", "v180", "v181", "v182", "v183", "v184", "v185", "v186", "v187", "v188", "v189", "v190", "v191", "v192", "v193", "v194", "v195", "v196", "v197", "v198", "v199", "v200", "v201", "v202", "v203", "v204", "v205", "v206", "v207", "v208", "v209", "v210", "v211", "v212", "v213", "v214", "v215", "v216", "v217", "v218", "v219", "v220", "v221", "v222", "v223", "v224", "v225", "v226", "v227", "v228", "v229", "v230", "v231", "v232", "v233", "v234", "v235", "v236", "v237", "v238", "v239", "v242", "v243", "v244", "v245", "v246", "v247", "v248", "v249", "v250", "v251", "v252", "v253", "v254", "v255", "s52", "s53", "s54", "s55", "s68", "s69", "s70", "s71", "s60", "s61", "s62", "s63", "s64", "s65", "s66", "s67", "s92", "s93", "s94", "s95", "s96", "s97");
  }
}

extern "C" void kernel_launch(void* const* d_in, const int* in_sizes, int n_in, void* d_out, int out_size, void* d_ws, size_t ws_size,
                              hipStream_t stream) {
  (void)n_in; (void)out_size; (void)d_ws; (void)ws_size;
  const int nseq = in_sizes[3] - 1, T = in_sizes[0] / (H * D);
  if (nseq < 1) return;
  const int maxitems = nseq + (T > nseq ? (T - nseq) / (nseq <= 8 ? 256 : 128) : 0);
  const int rounds = maxitems > GPH ? (maxitems + GPH - 1) / GPH : 1;
  varlen_attn_kernel<<<NWG * rounds, 512, 0, stream>>>((const char*)d_in[0], (const char*)d_in[1], (const char*)d_in[2], (const int*)d_in[3], (char*)d_out, nseq, 2 * T
  );
}
```
